```python
import torch

def _softmax(x, axis=-1):
    return torch.softmax(x, dim=axis)
def _gelu(x):
    return torch.nn.functional.gelu(x, approximate="tanh")
def _silu(x):
    return torch.nn.functional.silu(x)
def _sigmoid(x):
    return torch.sigmoid(x)
def _layernorm(x, axis=-1, eps=1e-5):
    m = x.mean(dim=axis, keepdim=True)
    v = x.var(dim=axis, keepdim=True, unbiased=False)
    return (x - m) / torch.sqrt(v + eps)
def _scatter_add(tgt, idx, src, axis=0):
    return tgt.clone().index_add_(axis, idx, src)
def _segment_sum(src, seg, n):
    out = torch.zeros((n,) + src.shape[1:], dtype=src.dtype)
    return out.index_add_(0, seg, src)
def _pad(x, pw):
    flat = tuple(v for pair in reversed(pw) for v in pair)
    return torch.nn.functional.pad(x, flat)

kernel_name = 'standalone__softmax_full_causal__prompt'

def setup_inputs():

    _w = (lambda a: (lambda t: t.to(torch.bfloat16) if t.is_floating_point() else t)(torch.as_tensor(a)))
    B, H, S, D = 1, 32, 2048, 128
    return {
        "q": _w((lambda *s: torch.randn(*s, dtype=torch.bfloat16) * 0.8)(B, H, S, D)),
        "k": _w((lambda *s: torch.randn(*s, dtype=torch.bfloat16) * 0.8)(B, H, S, D)),
        "v": _w((lambda *s: torch.randn(*s, dtype=torch.bfloat16) * 0.05)(B, H, S, D)),
    }

def reference(q, k, v):
    B, H, S, D = q.shape[0], q.shape[1], q.shape[2], q.shape[3]
    scale = D ** -0.5
    scores = torch.einsum('bhqd,bhkd->bhqk', q, k) * scale
    qi = torch.reshape(torch.arange(S), (1, 1, S, 1))
    kj = torch.reshape(torch.arange(S), (1, 1, 1, S))
    causal_bias = (lambda x, dt: x.to(dt))(kj > qi, scores.dtype) * (-1e9)
    attn = _softmax(scores + causal_bias, axis=-1)
    return torch.einsum('bhqk,bhkd->bhqd', attn, v)
```

```cpp
#include <hip/hip_runtime.h>
#include <hip/hip_bf16.h>
#include <cstdio>
#include <cstdint>

enum { ORDER_NATURAL = 0, ORDER_REVERSED = 1, ORDER_PAIRED = 2, ORDER_XCD = 4 };
using TIN  = __hip_bfloat16;
using TOUT = __hip_bfloat16;
constexpr int B = 1, H = 32, HKV = 32, SQ = 2048, SKV = 2048, D = 128;
constexpr int QOFF = 0;
constexpr int WINDOW = SKV;
constexpr float THR = 8.f;
constexpr int ORDER = ORDER_REVERSED | ORDER_XCD;
constexpr int GRID_WGS = 0;
constexpr bool WSKIP = WINDOW <= 512;
constexpr float SCALE = 0.08838834764831845f;
constexpr int NW = 8, QBLK = 32, KVBLK = 64, QB = NW * QBLK;
constexpr int SHM_V = KVBLK * D * 2, SHM_K = KVBLK * D * 2;
constexpr int LDS_BYTES = 2 * SHM_V + 2 * SHM_K + NW * 64 * 4;
static_assert(D == 128 && SQ % QB == 0 && SKV % KVBLK == 0 && H % HKV == 0 && QOFF >= 0 && QOFF + SQ <= SKV && WINDOW >= 1, "geometry");

using bf16 = __hip_bfloat16;
typedef short bf16x8 __attribute__((ext_vector_type(8)));
typedef short s16x4 __attribute__((ext_vector_type(4)));
typedef float f32x16 __attribute__((ext_vector_type(16)));
typedef float f32x4 __attribute__((ext_vector_type(4)));
typedef unsigned u32x4 __attribute__((ext_vector_type(4)));
template <class A, class Bt> struct same_t { static constexpr bool v = false; };
template <class A> struct same_t<A, A> { static constexpr bool v = true; };

#define KSWZ(row, colB) ((row) * 256 + ((colB) ^ (((row) & 7) << 4)))
#define SBAR() __builtin_amdgcn_sched_barrier(0)
__device__ __forceinline__ int v_st(int k, int c) { const int kk = (k & ~0xC) | ((k & 4) << 1) | ((k & 8) >> 1); return ((kk >> 3) * 4 + (c >> 5)) * 512 + ((kk & 7) * 32 + (c & 31)) * 2; }
__device__ __forceinline__ int v_rd_base(int lane) { return ((lane & 3) << 3) | (((lane >> 2) & 3) << 6) | (((lane >> 4) & 1) << 5) | (((lane >> 5) & 1) << 8); }
constexpr int v_rd_off(int d0, int ks, int half) { return d0 * 512 + ks * 4096 + half * 2048; }
__device__ __forceinline__ int crow(int r, int hi) { return (r & 3) + 8 * (r >> 2) + 4 * hi; }
__device__ __forceinline__ unsigned cvtpk(float lo, float hi) {
    unsigned r; asm volatile("v_cvt_pk_bf16_f32 %0, %1, %2" : "=v"(r) : "v"(lo), "v"(hi)); return r;
}
__device__ __forceinline__ bf16x8 pack8(f32x4 a, f32x4 b) {
    u32x4 w = {cvtpk(a[0], a[1]), cvtpk(a[2], a[3]), cvtpk(b[0], b[1]), cvtpk(b[2], b[3])};
    return *reinterpret_cast<bf16x8*>(&w);
}
template <class T> __device__ __forceinline__ bf16x8 load8(const T* p) {
    if constexpr (same_t<T, float>::v) { return pack8(*(const f32x4*)p, *(const f32x4*)(p + 4)); }
    else { return *reinterpret_cast<const bf16x8*>(p); }
}
__device__ __forceinline__ void mask_tile(f32x16& p0, f32x16& p1, int dq, unsigned W) {
    const float NEG = -__builtin_inff();
#pragma unroll
    for (int r = 0; r < 16; ++r) {
        const int c = (r & 3) + 8 * (r >> 2);
        if ((unsigned)(dq - c) >= W) p0[r] = NEG;
        if ((unsigned)(dq - c - 32) >= W) p1[r] = NEG;
    }
}
__device__ __forceinline__ void partialSM(f32x16& p0, f32x16& p1, float& m_reg, float& mn, float& alpha) {
    float pmax = p0[0]; for (int r = 1; r < 16; ++r) pmax = fmaxf(pmax, p0[r]); for (int r = 0; r < 16; ++r) pmax = fmaxf(pmax, p1[r]);
    { auto rr = __builtin_amdgcn_permlane32_swap(__float_as_uint(pmax), __float_as_uint(pmax), false, false);
      pmax = fmaxf(__uint_as_float(rr[0]), __uint_as_float(rr[1])); }
    constexpr float C2 = 1.4426950408889634f * SCALE;
    if (__builtin_expect(__all((pmax - m_reg) * SCALE <= THR), 1)) { mn = m_reg; alpha = 1.f; }
    else { mn = fmaxf(m_reg, pmax); alpha = __builtin_amdgcn_exp2f((m_reg - mn) * C2); m_reg = mn; }
    const float mnL = -mn * C2;
    for (int r = 0; r < 16; ++r) p0[r] = fmaf(p0[r], C2, mnL); for (int r = 0; r < 16; ++r) p1[r] = fmaf(p1[r], C2, mnL);
    for (int r = 0; r < 16; ++r) p0[r] = __builtin_amdgcn_exp2f(p0[r]);
}
__device__ __forceinline__ void finishSM(f32x16& p0, f32x16& p1, float alpha, float& l_reg, bf16x8& pa0, bf16x8& pa1, bf16x8& pa2, bf16x8& pa3) {
    for (int r = 0; r < 16; ++r) p1[r] = __builtin_amdgcn_exp2f(p1[r]);
    float ps = 0; for (int r = 0; r < 16; ++r) ps += p0[r]; for (int r = 0; r < 16; ++r) ps += p1[r];
    { auto rr = __builtin_amdgcn_permlane32_swap(__float_as_uint(ps), __float_as_uint(ps), false, false);
      ps = __uint_as_float(rr[0]) + __uint_as_float(rr[1]); }
    l_reg = l_reg * alpha + ps;
#define PK4(P, B_, OUT) do { unsigned a0 = cvtpk(P[B_+0], P[B_+1]), a1 = cvtpk(P[B_+2], P[B_+3]);                          \
        unsigned b0 = cvtpk(P[B_+4], P[B_+5]), b1 = cvtpk(P[B_+6], P[B_+7]);                                             \
        auto r0 = __builtin_amdgcn_permlane32_swap(a0, b0, false, false); auto r1 = __builtin_amdgcn_permlane32_swap(a1, b1, false, false); \
        u32x4 w = {r0[0], r1[0], r0[1], r1[1]}; OUT = *reinterpret_cast<bf16x8*>(&w); } while (0)
    PK4(p0, 0, pa0); PK4(p0, 8, pa1); PK4(p1, 0, pa2); PK4(p1, 8, pa3);
#undef PK4
}
template <int KB, bool SK>
__device__ __forceinline__ void qkt(f32x16& p0, f32x16& p1, const char* K_lds, int r32, int hi, const bf16x8* qr, bool act) {
    if (SK && !act) { const float NEG = -__builtin_inff();
#pragma unroll
        for (int r = 0; r < 16; ++r) { p0[r] = NEG; p1[r] = NEG; } return; }
    p0 = f32x16{}; p1 = f32x16{};
    const char* kb[4];
#pragma unroll
    for (int dd = 0; dd < 4; ++dd) kb[dd] = K_lds + KB * SHM_K + KSWZ(r32, (dd * 16 + hi * 8) * 2);
#pragma unroll
    for (int d0 = 0; d0 < 8; ++d0) { const char* a = kb[d0 & 3] + (d0 >> 2) * 128;
        bf16x8 b0 = *reinterpret_cast<const bf16x8*>(a);
        bf16x8 b1 = *reinterpret_cast<const bf16x8*>(a + 32 * 256);
        p0 = __builtin_amdgcn_mfma_f32_32x32x16_bf16(b0, qr[d0], p0, 0, 0, 0);
        p1 = __builtin_amdgcn_mfma_f32_32x32x16_bf16(b1, qr[d0], p1, 0, 0, 0); }
}
template <int VB, bool SK>
__device__ __forceinline__ void pv_tile(f32x16* o, int vb0, bf16x8 pa0, bf16x8 pa1, bf16x8 pa2, bf16x8 pa3, bool act) {
    if (SK && !act) return;
#define TRRD(dst, off) asm volatile("ds_read_b64_tr_b16 %0, %1 offset:%2" : "=&v"(dst) : "v"(vb0), "i"(off) : "memory")
#define PV_D0(d0) do { s16x4 l0, l1, l2, l3, h0, h1, h2, h3; constexpr int b_ = VB * SHM_V + v_rd_off(d0, 0, 0);     \
        TRRD(l0, b_); TRRD(h0, b_ + 2048); TRRD(l1, b_ + 4096); TRRD(h1, b_ + 6144); TRRD(l2, b_ + 8192); TRRD(h2, b_ + 10240); TRRD(l3, b_ + 12288); TRRD(h3, b_ + 14336); \
        asm volatile("s_waitcnt lgkmcnt(0)" ::: "memory"); SBAR();                 \
        o[d0] = __builtin_amdgcn_mfma_f32_32x32x16_bf16(pa0, (bf16x8){l0[0], l0[1], l0[2], l0[3], h0[0], h0[1], h0[2], h0[3]}, o[d0], 0, 0, 0);   \
        o[d0] = __builtin_amdgcn_mfma_f32_32x32x16_bf16(pa1, (bf16x8){l1[0], l1[1], l1[2], l1[3], h1[0], h1[1], h1[2], h1[3]}, o[d0], 0, 0, 0);   \
        o[d0] = __builtin_amdgcn_mfma_f32_32x32x16_bf16(pa2, (bf16x8){l2[0], l2[1], l2[2], l2[3], h2[0], h2[1], h2[2], h2[3]}, o[d0], 0, 0, 0);   \
        o[d0] = __builtin_amdgcn_mfma_f32_32x32x16_bf16(pa3, (bf16x8){l3[0], l3[1], l3[2], l3[3], h3[0], h3[1], h3[2], h3[3]}, o[d0], 0, 0, 0); } while (0)
    PV_D0(0); PV_D0(1); PV_D0(2); PV_D0(3);
#undef PV_D0
#undef TRRD
}

template <class TIn, class TOut> struct BlockRef { const TIn* Q; const TIn* K; const TIn* V; TOut* O; int P0; };
template <class TIn> struct Seam {
    bf16x8 qr[8];
    bf16x8 st_v0, st_v1, st_k0, st_k1; f32x4 sf0, sf1, sf2, sf3;
    f32x4 tq[16];
};
__device__ __forceinline__ int swa_jlo(int P0, int W) { const int lowk = P0 - W + 1; return lowk > 0 ? lowk / KVBLK : 0; }
#define ROW(p, k0, rr) ((p) + (size_t)((k0) + (rr)) * D + sc)
#define VMW() asm volatile("s_waitcnt vmcnt(0)" ::: "memory")
#define VMWN(n) asm volatile("s_waitcnt vmcnt(%0)" :: "i"(n) : "memory")
#define SLOAD_H(Kp, Vp, k0) do { S.st_v0 = load8<TIn>(ROW(Vp, k0, sr)); S.st_v1 = load8<TIn>(ROW(Vp, k0, 32 + sr));              \
                         S.st_k0 = load8<TIn>(ROW(Kp, k0, sr)); S.st_k1 = load8<TIn>(ROW(Kp, k0, 32 + sr)); } while (0)
#define SWRITE_HK(bf) do { *(bf16x8*)(K_lds + (bf) * SHM_K + kws) = S.st_k0; *(bf16x8*)(K_lds + (bf) * SHM_K + kws + 32 * 256) = S.st_k1; } while (0)
#define SWRITE_HV(bf) do { *(bf16x8*)(V_lds + (bf) * SHM_V + vst0) = S.st_v0; *(bf16x8*)(V_lds + (bf) * SHM_V + vst1) = S.st_v1; } while (0)
#define SWRITE_H(bf) do { SWRITE_HV(bf); SWRITE_HK(bf); } while (0)
#define SLOAD_F(p, k0) do { S.sf0 = *(const f32x4*)ROW(p, k0, sr); S.sf1 = *(const f32x4*)(ROW(p, k0, sr) + 4);                \
                            S.sf2 = *(const f32x4*)ROW(p, k0, 32 + sr); S.sf3 = *(const f32x4*)(ROW(p, k0, 32 + sr) + 4); } while (0)
#define SWRITE_KF(bf) do { *(bf16x8*)(K_lds + (bf) * SHM_K + kws) = pack8(S.sf0, S.sf1); *(bf16x8*)(K_lds + (bf) * SHM_K + kws + 32 * 256) = pack8(S.sf2, S.sf3); } while (0)
#define SWRITE_VF(bf) do { *(bf16x8*)(V_lds + (bf) * SHM_V + vst0) = pack8(S.sf0, S.sf1); *(bf16x8*)(V_lds + (bf) * SHM_V + vst1) = pack8(S.sf2, S.sf3); } while (0)
template <class TIn, class TOut>
__device__ __forceinline__ void causal_swa_prime(const BlockRef<TIn, TOut>& cur, int W, char* lds, Seam<TIn>& S) {
    constexpr bool F32 = same_t<TIn, float>::v;
    const int tid = threadIdx.x, wid = __builtin_amdgcn_readfirstlane(tid >> 6), lane = tid & 63, r32 = lane & 31, hi = lane >> 5;
    const int sr = tid >> 4, sc = (tid & 15) * 8, kws = KSWZ(sr, sc * 2); char* K_lds = lds + 2 * SHM_V;
    const int kb0 = swa_jlo(cur.P0, W) * KVBLK;
    for (int d0 = 0; d0 < 8; ++d0) S.qr[d0] = load8<TIn>(cur.Q + (size_t)(wid * QBLK + r32) * D + d0 * 16 + hi * 8);
    if constexpr (F32) { SLOAD_F((const float*)cur.K, kb0); VMW(); SWRITE_KF(0); SBAR(); SLOAD_F((const float*)cur.V, kb0); }
    else { SLOAD_H(cur.K, cur.V, kb0); VMW(); SWRITE_HK(0); }
    __syncthreads();
}
template <class TIn, class TOut>
__device__ __forceinline__ void causal_swa_block(const BlockRef<TIn, TOut>& cur, const BlockRef<TIn, TOut>& nxt, int skv, int W, char* lds, Seam<TIn>& S) {
    constexpr bool F32 = same_t<TIn, float>::v;
    const int tid = threadIdx.x, wid = __builtin_amdgcn_readfirstlane(tid >> 6), lane = tid & 63, r32 = lane & 31, hi = lane >> 5;
    const int j_lo = swa_jlo(cur.P0, W);
    int j_hi = (cur.P0 + QB - 1) / KVBLK + 1; if (j_hi > skv / KVBLK) j_hi = skv / KVBLK;
    const int NT = j_hi - j_lo;
    const int kbn = swa_jlo(nxt.P0, W) * KVBLK;
    const int qlo = cur.P0 + wid * QBLK, qm = qlo + r32 - 4 * hi;
    char* V_lds = lds; char* K_lds = lds + 2 * SHM_V;
    float* ws = (float*)(lds + 2 * SHM_V + 2 * SHM_K) + wid * 64; float* li_l = ws, * al_l = ws + 32;
    float m_reg = -1e30f, l_reg = 0; f32x16 o[4] = {};
    const int sr = tid >> 4, sc = (tid & 15) * 8, vst0 = v_st(sr, sc), vst1 = v_st(32 + sr, sc), kws = KSWZ(sr, sc * 2);
    const int vb0 = (int)(uintptr_t)V_lds + v_rd_base(lane);
    const TIn* Kh = cur.K; const TIn* Vh = cur.V;
#define RESC(a) do { if (__any((a) < 1.f)) { if (hi == 0) al_l[r32] = (a); asm volatile("s_waitcnt lgkmcnt(0)" ::: "memory");              \
                     for (int d_ = 0; d_ < 4; ++d_) for (int r = 0; r < 16; ++r) o[d_][r] *= al_l[crow(r, hi)]; } } while (0)
#define KBASE(t) ((j_lo + (t)) * KVBLK)
#define ACT(t) (KBASE(t) <= qlo + QBLK - 1 && KBASE(t) + KVBLK - 1 >= qlo - W + 1)
#define MASKT(P0_, P1_, t) do { const int kb_ = KBASE(t); if ((!SK || ACT(t)) && (kb_ + KVBLK - 1 > qlo || kb_ <= qlo + QBLK - 1 - W)) mask_tile(P0_, P1_, qm - kb_, (unsigned)W); } while (0)
    constexpr int NQL = F32 ? 16 : 8;
    constexpr bool SK = WSKIP && !F32;
#define SEAM_K0() do { VMWN(NQL); if constexpr (F32) { SWRITE_KF(0); SBAR(); SLOAD_F((const float*)nxt.V, kbn); } else { SWRITE_HK(0); } SBAR(); } while (0)
    f32x16 pA0, pA1, pB0, pB1; float mnA, mnB, alA, alB; bf16x8 pa0, pa1, pa2, pa3;
    if constexpr (F32) { VMW(); SWRITE_VF(0); SBAR(); } else { SWRITE_HV(0); SBAR(); }
    if (NT > 1) { if constexpr (F32) SLOAD_F((const float*)Kh, KBASE(1)); else SLOAD_H(Kh, Vh, KBASE(1)); }
    SBAR(); qkt<0, SK>(pA0, pA1, K_lds, r32, hi, S.qr, ACT(0));
    if constexpr (F32) { if (NT > 1) { VMW(); SWRITE_KF(1); SBAR(); SLOAD_F((const float*)Vh, KBASE(1)); } }
    MASKT(pA0, pA1, 0); partialSM(pA0, pA1, m_reg, mnA, alA);
    if (NT > 1) { VMW(); if constexpr (F32) { SWRITE_VF(1); SBAR(); if (NT > 2) SLOAD_F((const float*)Kh, KBASE(2)); } else SWRITE_H(1); }
    __syncthreads();
#define HALF_STEP(PX0, PX1, mnX, alX, PY0, PY1, alY, t, KB, VB, SB) do {                                                      \
        SBAR(); qkt<KB, SK>(PX0, PX1, K_lds, r32, hi, S.qr, ACT(t));                                             \
        finishSM(PY0, PY1, alY, l_reg, pa0, pa1, pa2, pa3); SBAR();                                                           \
        if ((t) + 1 < NT) { if constexpr (F32) { VMW(); SWRITE_KF(SB); SBAR(); SLOAD_F((const float*)Vh, KBASE((t) + 1)); }  \
                            else { SLOAD_H(Kh, Vh, KBASE((t) + 1)); } SBAR(); }                                               \
        pv_tile<VB, SK>(o, vb0, pa0, pa1, pa2, pa3, ACT((t) - 1)); MASKT(PX0, PX1, (t)); partialSM(PX0, PX1, m_reg, mnX, alX);                                        \
        __syncthreads();                                                                                                      \
        if ((t) + 1 < NT) { VMW(); if constexpr (F32) { SWRITE_VF(SB); SBAR(); if ((t) + 2 < NT) SLOAD_F((const float*)Kh, KBASE((t) + 2)); } \
                            else { SWRITE_H(SB); } }                                                                          \
        RESC(alX); __syncthreads(); } while (0)
    for (int t = 1; t + 1 < NT; t += 2) {
        HALF_STEP(pB0, pB1, mnB, alB, pA0, pA1, alA, t, 1, 0, 0);
        HALF_STEP(pA0, pA1, mnA, alA, pB0, pB1, alB, t + 1, 0, 1, 1);
    }
    const bool even = (NT & 1) == 0;
    if (even) { SBAR(); qkt<1, SK>(pB0, pB1, K_lds, r32, hi, S.qr, ACT(NT - 1)); SBAR(); }
#define QROW(e) (nxt.Q + (size_t)(wid * QBLK + r32) * D + ((e) >> 1) * 16 + hi * 8 + ((e) & 1) * 4)
    if constexpr (F32) { SLOAD_F((const float*)nxt.K, kbn); SBAR();
#pragma unroll
        for (int e = 0; e < 8; ++e) S.tq[e] = *(const f32x4*)QROW(e); }
    else { SLOAD_H(nxt.K, nxt.V, kbn); SBAR();
#pragma unroll
        for (int d0 = 0; d0 < 8; ++d0) S.qr[d0] = load8<TIn>(nxt.Q + (size_t)(wid * QBLK + r32) * D + d0 * 16 + hi * 8); }
    SBAR();
    finishSM(pA0, pA1, alA, l_reg, pa0, pa1, pa2, pa3); SBAR();
    if constexpr (F32) {
#pragma unroll
        for (int e = 8; e < 16; ++e) S.tq[e] = *(const f32x4*)QROW(e); SBAR(); }
#undef QROW
    pv_tile<0, SK>(o, vb0, pa0, pa1, pa2, pa3, ACT(even ? NT - 2 : NT - 1));
    if (even) { MASKT(pB0, pB1, NT - 1); partialSM(pB0, pB1, m_reg, mnB, alB); __syncthreads(); RESC(alB);
        finishSM(pB0, pB1, alB, l_reg, pa0, pa1, pa2, pa3); SBAR(); pv_tile<1, SK>(o, vb0, pa0, pa1, pa2, pa3, ACT(NT - 1)); }
    SBAR(); SEAM_K0();
    if (hi == 0) li_l[r32] = l_reg; asm volatile("s_waitcnt lgkmcnt(0)" ::: "memory");
    float rli[16];
#pragma unroll
    for (int r = 0; r < 16; ++r) rli[r] = __builtin_amdgcn_rcpf(li_l[crow(r, hi)]);
    TOut* Ow = cur.O + (size_t)(wid * QBLK) * D;
#pragma unroll
    for (int r = 0; r < 16; ++r) { const int orow = crow(r, hi);
#pragma unroll
        for (int d0 = 0; d0 < 4; ++d0) { const float v = o[d0][r] * rli[r];
            if constexpr (same_t<TOut, float>::v) { Ow[(size_t)orow * D + d0 * 32 + r32] = v; }
            else { const float vn = __shfl_xor(v, 1);
                   if ((r32 & 1) == 0) *(unsigned*)(Ow + (size_t)orow * D + d0 * 32 + r32) = cvtpk(v, vn); } } }
    if constexpr (F32) {
#pragma unroll
        for (int d0 = 0; d0 < 8; ++d0) S.qr[d0] = pack8(S.tq[2 * d0], S.tq[2 * d0 + 1]); }
    __syncthreads();
#undef RESC
#undef KBASE
#undef ACT
#undef MASKT
#undef SEAM_K0
#undef HALF_STEP
}
#undef ROW
#undef VMW
#undef VMWN
#undef SLOAD_H
#undef SWRITE_HK
#undef SWRITE_HV
#undef SWRITE_H
#undef SLOAD_F
#undef SWRITE_KF
#undef SWRITE_VF

__host__ __device__ inline int swa_nramp(int nqb, int W, int qoff) { const int t = W - 1 - qoff; const int n = t < 0 ? 0 : t / QB + 1; return n > nqb ? nqb : n; }
__host__ __device__ inline int swa_nx(int nqb, int nramp, int order) { return (order & ORDER_PAIRED) ? (nramp + 1) / 2 + (nqb - nramp) : nqb; }
struct SwaItem { int bh, qb0, qb1; };
__device__ __forceinline__ SwaItem swa_decode(int L, int nb, int nh, int nhkv, int nqb, int nx, int nramp, int order) {
    const int G = nh / nhkv; SwaItem it; int x;
    if ((order & ORDER_XCD) && (nb * nhkv) % 8 == 0) { const int xcd = L & 7, k = L >> 3, per = G * nx, gi = k / per, r = k - gi * per;
        it.bh = (gi * 8 + xcd) * G + r / nx; x = r % nx; }
    else { it.bh = L / nx; x = L - it.bh * nx; }
    if (order & ORDER_PAIRED) { const int ns = nqb - nramp;
        if (x < ns) { it.qb0 = it.qb1 = nqb - 1 - x; } else { it.qb0 = x - ns; it.qb1 = nramp - 1 - it.qb0; } }
    else { it.qb0 = it.qb1 = ((order & 3) == ORDER_REVERSED) ? nqb - 1 - x : x; }
    return it;
}
template <class TIn, class TOut>
__device__ __forceinline__ BlockRef<TIn, TOut> swa_ref(const SwaItem& it, int pass, const TIn* Q, const TIn* K, const TIn* V, TOut* O,
                                                    int nh, int nhkv, int sq, int skv, int qoff) {
    const int qb = pass ? it.qb1 : it.qb0, kvh = it.bh / (nh / nhkv);
    BlockRef<TIn, TOut> r;
    r.Q = Q + ((size_t)it.bh * sq + (size_t)qb * QB) * D; r.O = O + ((size_t)it.bh * sq + (size_t)qb * QB) * D;
    r.K = K + (size_t)kvh * skv * D; r.V = V + (size_t)kvh * skv * D; r.P0 = qoff + qb * QB;
    return r;
}
template <class TIn, class TOut>
__global__ __launch_bounds__(64 * NW, 1)
void attn_fwd_causal_swa(const TIn* __restrict__ Q, const TIn* __restrict__ K, const TIn* __restrict__ V, TOut* __restrict__ O,
                         const int* __restrict__ Wdev, int nb, int nh, int nhkv, int sq, int skv, int W, int qoff, int order, int nramp) {
    extern __shared__ __attribute__((aligned(16))) char lds[];
    if (Wdev) W = *Wdev;
    const int nqb = sq / QB, nx = swa_nx(nqb, nramp, order), total = nx * nb * nh, stride = gridDim.x;
    int L = blockIdx.x; if (L >= total) return;
    SwaItem it = swa_decode(L, nb, nh, nhkv, nqb, nx, nramp, order); int pass = 0;
    BlockRef<TIn, TOut> cur = swa_ref<TIn, TOut>(it, 0, Q, K, V, O, nh, nhkv, sq, skv, qoff);
    Seam<TIn> S;
    causal_swa_prime<TIn, TOut>(cur, W, lds, S);
    for (;;) {
        const bool more_pass = pass == 0 && it.qb1 != it.qb0, more_item = L + stride < total, last = !more_pass && !more_item;
        SwaItem itn = it; int passn = pass + 1, Ln = L;
        if (!more_pass) { passn = 0; Ln = more_item ? L + stride : L; itn = swa_decode(Ln, nb, nh, nhkv, nqb, nx, nramp, order); }
        const BlockRef<TIn, TOut> nxt = last ? cur : swa_ref<TIn, TOut>(itn, passn, Q, K, V, O, nh, nhkv, sq, skv, qoff);
        causal_swa_block<TIn, TOut>(cur, nxt, skv, W, lds, S);
        if (last) break;
        cur = nxt; it = itn; pass = passn; L = Ln;
    }
}
template <class TIn, class TOut>
static void causal_swa_launch(const TIn* q, const TIn* k, const TIn* v, TOut* o, const int* Wdev  ,
                              int nb, int nh, int nhkv, int sq, int skv, int W  ,
                              int qoff, int order, hipStream_t stream) {
    int grid_wgs = 256;
    {
        hipError_t e = hipFuncSetAttribute((const void*)attn_fwd_causal_swa<TIn, TOut>, hipFuncAttributeMaxDynamicSharedMemorySize, LDS_BYTES);
        if (e != hipSuccess) fprintf(stderr, "attn_fwd_causal_swa: hipFuncSetAttribute(%d B LDS): %s\n", LDS_BYTES, hipGetErrorString(e));
        if (grid_wgs <= 0) { int dev = 0, cus = 0; (void)hipGetDevice(&dev);
            if (hipDeviceGetAttribute(&cus, hipDeviceAttributeMultiprocessorCount, dev) != hipSuccess || cus <= 0) cus = 256; grid_wgs = cus; } }
    const int nqb = sq / QB, nramp = swa_nramp(nqb, W, qoff), nx = swa_nx(nqb, nramp, order), total = nx * nb * nh;
    const int grid = total < grid_wgs ? total : grid_wgs;
    attn_fwd_causal_swa<TIn, TOut><<<dim3(grid), 64 * NW, LDS_BYTES, stream>>>(q, k, v, o, Wdev, nb, nh, nhkv, sq, skv, W, qoff, order, nramp);
}

extern "C" void kernel_launch(void* const* d_in, const int* in_sizes, int n_in, void* d_out, int out_size, void* d_ws,
                              size_t ws_size, hipStream_t stream) {
    (void)d_ws; (void)ws_size;
    constexpr long NQ = (long)B * H * SQ * D, NKV = (long)B * HKV * SKV * D;
    if ((n_in != 3 && n_in != 4) || in_sizes[0] != NQ || in_sizes[1] != NKV || in_sizes[2] != NKV || (n_in == 4 && in_sizes[3] != 1) || out_size != NQ) {
        fprintf(stderr, "attn_fwd_causal_swa: shape mismatch (n_in=%d in_sizes=%d,%d,%d,%d out=%d; want 3 or 4 / %ld,%ld,%ld[,1] / %ld)\n",
                n_in, n_in > 0 ? in_sizes[0] : -1, n_in > 1 ? in_sizes[1] : -1, n_in > 2 ? in_sizes[2] : -1, n_in > 3 ? in_sizes[3] : -1, out_size, NQ, NKV, NKV, NQ);
        return;
    }
    const int* Wdev = n_in == 4 ? (const int*)d_in[3] : nullptr;
    causal_swa_launch<TIN, TOUT>((const TIN*)d_in[0], (const TIN*)d_in[1], (const TIN*)d_in[2], (TOUT*)d_out, Wdev, B, H, HKV, SQ, SKV, WINDOW, QOFF, ORDER, stream);
}
```

```cpp
#include <hip/hip_runtime.h>
#include <hip/hip_bf16.h>

extern "C" __global__ __launch_bounds__(256, 1) void attn_causal_fwd(const void* q, const void* k, const void* v, void* o) {
  extern __shared__ __attribute__((aligned(16))) char lds[];
  const unsigned wid = __builtin_amdgcn_readfirstlane(threadIdx.x >> 6);
  asm volatile(
    "L_start_%=:\n\t"
    "s_load_dwordx8 s[20:27], %0, 0x0\n\t"
    "v_mbcnt_lo_u32_b32 v239, -1, 0\n\t"
    "v_mbcnt_hi_u32_b32 v239, -1, v239\n\t"
    "s_getpc_b64 s[76:77]\n\t"
    "v_lshlrev_b32 v242, 7, v239\n\t"
    "v_add_u32 v243, 0x2000, v242\n\t"
    "v_min_u32 v243, L_end_%=-L_start_%=-256, v243\n\t"
    "global_load_dword v241, v242, s[76:77]\n\t"
    "global_load_dword v241, v243, s[76:77]\n\t"
    "s_mov_b32 s74, %1\n\t"
    "s_and_b32 s76, %2, 7\n\t"
    "s_lshr_b32 s77, %2, 6\n\t"
    "s_lshl_b32 s76, s76, 2\n\t"
    "s_add_u32 s76, s76, s77\n\t"
    "s_lshl_b32 s82, s76, 19\n\t"
    "v_bfe_u32 v242, v239, 2, 3\n\t"
    "v_lshlrev_b32 v242, 8, v242\n\t"
    "v_lshrrev_b32 v243, 5, v239\n\t"
    "v_lshl_add_u32 v242, v243, 6, v242\n\t"
    "v_bfe_u32 v244, v239, 4, 1\n\t"
    "v_lshlrev_b32 v244, 1, v244\n\t"
    "v_and_b32 v245, 3, v239\n\t"
    "v_xor_b32 v244, v244, v245\n\t"
    "v_lshl_add_u32 v249, v244, 4, v242\n\t"
    "v_xor_b32 v251, 48, v249\n\t"
    "v_add_u32 v248, 896, v249\n\t"
    "v_add_u32 v250, 896, v251\n\t"
    "s_and_b32 s77, s74, 1\n\t"
    "s_lshl_b32 s30, s77, 12\n\t"
    "s_lshr_b32 s78, s74, 1\n\t"
    "s_lshl_b32 s78, s78, 15\n\t"
    "s_or_b32 s30, s30, s78\n\t"
    "s_mul_i32 s77, s77, 48\n\t"
    "s_lshl_b32 s78, s74, 11\n\t"
    "v_xor_b32 v236, s77, v249\n\t"
    "v_add_u32 v236, s78, v236\n\t"
    "v_add_u32 v235, 896, v236\n\t"
    "s_bfe_u32 s75, %2, 0x30003\n\t"
    "s_lshl_b32 s76, s75, 1\n\t"
    "s_add_u32 s77, s76, s74\n\t"
    "s_sub_u32 s78, 28, s76\n\t"
    "s_add_u32 s78, s78, s74\n\t"
    "s_cmp_lt_u32 s74, 2\n\t"
    "s_cselect_b32 s61, s77, s78\n\t"
    "s_cselect_b32 s83, 0, 0x8000\n\t"
    "s_lshl_b32 s76, s74, 14\n\t"
    "s_add_u32 s83, s83, s76\n\t"
    "s_add_u32 s83, s83, 65536\n\t"
    "s_lshl_b32 s76, s61, 14\n\t"
    "s_add_u32 s79, s76, s82\n\t"
    "s_mov_b32 s38, 279424\n\t"
    "s_mov_b32 s39, 0x20000\n\t"
    "s_mov_b32 s54, 17280\n\t"
    "s_mov_b32 s55, 0x20000\n\t"
    "s_add_u32 m0, s30, 24576\n\t"
    "s_mov_b32 s56, 0\n\t"
    "s_waitcnt lgkmcnt(0)\n\t"
    "s_add_u32 s36, s22, s82\n\t"
    "s_addc_u32 s37, s23, 0\n\t"
    "s_sub_u32 s36, s36, 896\n\t"
    "s_subb_u32 s37, s37, 0\n\t"
    "buffer_load_dwordx4 v235, s[36:39], s56 offen offset:0 lds\n\t"
    "buffer_load_dwordx4 v236, s[36:39], s56 offen offset:1024 lds\n\t"
    "s_add_u32 s52, s20, s79\n\t"
    "s_addc_u32 s53, s21, 0\n\t"
    "s_sub_u32 s52, s52, 896\n\t"
    "s_subb_u32 s53, s53, 0\n\t"
    "s_add_u32 m0, s83, 8192\n\t"
    "s_movk_i32 s76, 8192\n\t"
    "buffer_load_dwordx4 v248, s[52:55], s76 offen offset:0 nt lds\n\t"
    "buffer_load_dwordx4 v249, s[52:55], s76 offen offset:1024 nt lds\n\t"
    "buffer_load_dwordx4 v250, s[52:55], s76 offen offset:2048 nt lds\n\t"
    "buffer_load_dwordx4 v251, s[52:55], s76 offen offset:3072 nt lds\n\t"
    "s_add_u32 m0, s83, 12288\n\t"
    "s_movk_i32 s76, 12288\n\t"
    "buffer_load_dwordx4 v248, s[52:55], s76 offen offset:0 nt lds\n\t"
    "buffer_load_dwordx4 v249, s[52:55], s76 offen offset:1024 nt lds\n\t"
    "buffer_load_dwordx4 v250, s[52:55], s76 offen offset:2048 nt lds\n\t"
    "buffer_load_dwordx4 v251, s[52:55], s76 offen offset:3072 nt lds\n\t"
    "v_mov_b32 v228, 0\n\t"
    "v_mov_b32 v229, 0\n\t"
    "v_mov_b32 v230, 0\n\t"
    "v_mov_b32 v231, 0\n\t"
    "s_mov_b32 s80, 0x3e0293ee\n\t"
    "s_mov_b32 s81, 0x3e0293ee\n\t"
    "v_mfma_f32_32x32x16_bf16 v[160:175], v[228:231], v[228:231], 0\n\t"
    "v_and_b32 v242, 15, v239\n\t"
    "v_lshrrev_b32 v243, 4, v239\n\t"
    "v_lshlrev_b32 v244, 2, v243\n\t"
    "v_sub_u32 v238, v242, v244\n\t"
    "v_bfe_u32 v244, v239, 3, 1\n\t"
    "v_mfma_f32_32x32x16_bf16 v[176:191], v[228:231], v[228:231], 0\n\t"
    "v_bfe_u32 v245, v239, 2, 1\n\t"
    "v_xor_b32 v245, v245, v244\n\t"
    "v_lshl_add_u32 v245, v245, 1, v244\n\t"
    "v_xor_b32 v245, v245, v243\n\t"
    "v_and_b32 v246, 7, v239\n\t"
    "v_mfma_f32_32x32x16_bf16 v[192:207], v[228:231], v[228:231], 0\n\t"
    "v_lshlrev_b32 v246, 6, v246\n\t"
    "v_lshl_add_u32 v246, v245, 4, v246\n\t"
    "v_lshl_add_u32 v232, v244, 12, v246\n\t"
    "v_add_u32 v232, 0x6000, v232\n\t"
    "v_lshl_add_u32 v252, v244, 11, v246\n\t"
    "v_mfma_f32_32x32x16_bf16 v[212:227], v[228:231], v[228:231], 0\n\t"
    "v_accvgpr_write_b32 a132, v252\n\t"
    "v_add_u32 v252, s83, v252\n\t"
    "v_lshlrev_b32 v240, 4, v239\n\t"
    "v_bfe_u32 v245, v239, 4, 1\n\t"
    "v_lshrrev_b32 v244, 5, v239\n\t"
    "v_mfma_f32_32x32x16_bf16 a[96:111], v[228:231], v[228:231], 0\n\t"
    "v_mov_b32 v237, 0x6000\n\t"
    "v_xor_b32 v246, v245, v244\n\t"
    "v_lshl_add_u32 v246, v246, 1, v244\n\t"
    "v_bfe_u32 v247, v239, 1, 1\n\t"
    "v_xor_b32 v246, v246, v247\n\t"
    "v_mfma_f32_32x32x16_bf16 a[112:127], v[228:231], v[228:231], 0\n\t"
    "v_bfe_u32 v247, v239, 2, 3\n\t"
    "v_lshlrev_b32 v247, 6, v247\n\t"
    "v_lshl_add_u32 v247, v246, 4, v247\n\t"
    "v_lshl_add_u32 v247, v244, 12, v247\n\t"
    "v_and_b32 v246, 1, v239\n\t"
    "v_mfma_f32_32x32x16_bf16 v[64:79], v[228:231], v[228:231], 0\n\t"
    "v_lshl_add_u32 v233, v246, 3, v247\n\t"
    "v_xor_b32 v234, 32, v233\n\t"
    "v_mov_b32 v208, 0\n\t"
    "v_mov_b32 v209, 0\n\t"
    "v_mov_b32 v210, 0\n\t"
    "v_mfma_f32_32x32x16_bf16 v[80:95], v[228:231], v[228:231], 0\n\t"
    "v_mov_b32 v211, 0\n\t"
    "v_and_b32 v246, 3, v239\n\t"
    "v_cmp_eq_u32 vcc, 0, v246\n\t"
    "v_mov_b32 v247, 0x3f803f80\n\t"
    "s_nop 0\n\t"
    "v_mfma_f32_32x32x16_bf16 v[96:111], v[228:231], v[228:231], 0\n\t"
    "v_cndmask_b32 v246, 0, v247, vcc\n\t"
    "v_accvgpr_write_b32 a128, v246\n\t"
    "v_accvgpr_write_b32 a129, v246\n\t"
    "v_accvgpr_write_b32 a130, v246\n\t"
    "v_accvgpr_write_b32 a131, v246\n\t"
    "v_mfma_f32_32x32x16_bf16 v[112:127], v[228:231], v[228:231], 0\n\t"
    "s_lshl_b32 s76, s74, 10\n\t"
    "s_add_u32 s76, s76, 114688\n\t"
    "s_xor_b32 s77, s76, 3072\n\t"
    "v_add_u32 v253, s76, v240\n\t"
    "v_add_u32 v254, s77, v240\n\t"
    "s_add_u32 s40, s24, s82\n\t"
    "s_addc_u32 s41, s25, 0\n\t"
    "s_sub_u32 s40, s40, 11136\n\t"
    "s_subb_u32 s41, s41, 0\n\t"
    "s_add_u32 s44, s22, s82\n\t"
    "s_addc_u32 s45, s23, 0\n\t"
    "s_add_u32 s44, s44, 269440\n\t"
    "s_addc_u32 s45, s45, 0\n\t"
    "s_add_u32 s48, s24, s82\n\t"
    "s_addc_u32 s49, s25, 0\n\t"
    "s_add_u32 s48, s48, 275584\n\t"
    "s_addc_u32 s49, s49, 0\n\t"
    "s_mov_b32 s43, 0x20000\n\t"
    "s_mov_b32 s47, 0x20000\n\t"
    "s_mov_b32 s51, 0x20000\n\t"
    "s_mov_b32 s42, 289664\n\t"
    "s_lshl_b32 s76, s75, 15\n\t"
    "s_sub_u32 s46, 254848, s76\n\t"
    "s_sub_u32 s50, 248704, s76\n\t"
    "s_mov_b32 s64, 0x4a800000\n\t"
    "s_getpc_b64 s[70:71]\n\t"
    "L_pc_%=:\n\t"
    "s_add_u32 s70, s70, L_loop_%=-L_pc_%=\n\t"
    "s_addc_u32 s71, s71, 0\n\t"
    "s_lshl_b32 s58, s61, 14\n\t"
    "s_add_u32 s58, s58, 32768\n\t"
    "s_min_u32 s58, s58, 311296\n\t"
    "s_mov_b32 s76, L_final_%=-L_loop_%=\n\t"
    "s_cmp_lt_u32 s58, 311296\n\t"
    "s_cselect_b32 s76, L_hdiag_%=-L_loop_%=, s76\n\t"
    "s_add_u32 s62, s70, s76\n\t"
    "s_addc_u32 s63, s71, 0\n\t"
    "s_mov_b32 s76, L_hymask_%=-L_loop_%=\n\t"
    "s_cmp_lt_u32 s74, 2\n\t"
    "s_cselect_b32 s72, L_switch_%=-L_loop_%=, s76\n\t"
    "s_lshl_b32 s69, s61, 6\n\t"
    "s_add_u32 s68, s69, 32\n\t"
    "s_add_u32 m0, s83, 0\n\t"
    "s_movk_i32 s76, 0\n\t"
    "buffer_load_dwordx4 v248, s[52:55], s76 offen offset:0 nt lds\n\t"
    "buffer_load_dwordx4 v249, s[52:55], s76 offen offset:1024 nt lds\n\t"
    "buffer_load_dwordx4 v250, s[52:55], s76 offen offset:2048 nt lds\n\t"
    "buffer_load_dwordx4 v251, s[52:55], s76 offen offset:3072 nt lds\n\t"
    "s_add_u32 m0, s83, 4096\n\t"
    "s_movk_i32 s76, 4096\n\t"
    "buffer_load_dwordx4 v248, s[52:55], s76 offen offset:0 nt lds\n\t"
    "buffer_load_dwordx4 v249, s[52:55], s76 offen offset:1024 nt lds\n\t"
    "buffer_load_dwordx4 v250, s[52:55], s76 offen offset:2048 nt lds\n\t"
    "buffer_load_dwordx4 v251, s[52:55], s76 offen offset:3072 nt lds\n\t"
    "s_cmp_lg_u32 s61, 0\n\t"
    "s_cbranch_scc1 L_nomask0_%=\n\t"
    "v_cvt_f32_i32 v242, v238\n\t"
    "v_add_f32 v242, 0.5, v242\n\t"
    "v_ldexp_f32 v242, v242, 64\n\t"
    "v_mov_b32 v246, 0xdf000000\n\t"
    "v_add_f32 v243, 0xdf800000, v242\n\t"
    "v_add_f32 v244, 0xe0000000, v242\n\t"
    "v_add_f32 v245, 0xe0400000, v242\n\t"
    "v_min_f32 v176, v230, v242\n\t"
    "v_mov_b32 v180, v246\n\t"
    "v_min_f32 v188, v231, v242\n\t"
    "v_min_f32 v177, v230, v243\n\t"
    "v_mov_b32 v181, v246\n\t"
    "v_min_f32 v189, v231, v243\n\t"
    "v_min_f32 v178, v230, v244\n\t"
    "v_mov_b32 v182, v246\n\t"
    "v_min_f32 v190, v231, v244\n\t"
    "v_min_f32 v179, v230, v245\n\t"
    "v_mov_b32 v183, v246\n\t"
    "v_min_f32 v191, v231, v245\n\t"
    "L_nomask0_%=:\n\t"
    "s_waitcnt vmcnt(12)\n\t"
    "s_barrier\n\t"
    "ds_read_b128 a[64:67], v232 offset:0\n\t"
    "ds_read_b128 a[68:71], v232 offset:512\n\t"
    "ds_read_b128 a[72:75], v232 offset:1024\n\t"
    "ds_read_b128 a[76:79], v232 offset:1536\n\t"
    "ds_read_b128 a[80:83], v232 offset:32768\n\t"
    "ds_read_b128 a[84:87], v232 offset:33280\n\t"
    "ds_read_b128 a[88:91], v232 offset:33792\n\t"
    "ds_read_b128 a[92:95], v232 offset:34304\n\t"
    "ds_read_b128 v[0:3], v252 offset:8192\n\t"
    "ds_read_b128 v[4:7], v252 offset:8704\n\t"
    "ds_read_b128 v[8:11], v252 offset:9216\n\t"
    "ds_read_b128 v[12:15], v252 offset:9728\n\t"
    "s_waitcnt lgkmcnt(3)\n\t"
    "v_lshlrev_b32 v16, 16, v0\n\t"
    "v_and_b32 v17, 0xffff0000, v0\n\t"
    "v_lshlrev_b32 v18, 16, v1\n\t"
    "v_and_b32 v19, 0xffff0000, v1\n\t"
    "v_lshlrev_b32 v20, 16, v2\n\t"
    "v_and_b32 v21, 0xffff0000, v2\n\t"
    "v_lshlrev_b32 v22, 16, v3\n\t"
    "v_and_b32 v23, 0xffff0000, v3\n\t"
    "v_pk_mul_f32 v[16:17], v[16:17], s[80:81]\n\t"
    "v_pk_mul_f32 v[18:19], v[18:19], s[80:81]\n\t"
    "v_pk_mul_f32 v[20:21], v[20:21], s[80:81]\n\t"
    "v_pk_mul_f32 v[22:23], v[22:23], s[80:81]\n\t"
    "v_cvt_pk_bf16_f32 v0, v16, v17\n\t"
    "v_cvt_pk_bf16_f32 v1, v18, v19\n\t"
    "v_cvt_pk_bf16_f32 v2, v20, v21\n\t"
    "v_cvt_pk_bf16_f32 v3, v22, v23\n\t"
    "v_accvgpr_write_b32 a0, v0\n\t"
    "v_accvgpr_write_b32 a1, v1\n\t"
    "v_accvgpr_write_b32 a2, v2\n\t"
    "v_accvgpr_write_b32 a3, v3\n\t"
    "s_waitcnt lgkmcnt(2)\n\t"
    "v_lshlrev_b32 v16, 16, v4\n\t"
    "v_and_b32 v17, 0xffff0000, v4\n\t"
    "v_lshlrev_b32 v18, 16, v5\n\t"
    "v_and_b32 v19, 0xffff0000, v5\n\t"
    "v_lshlrev_b32 v20, 16, v6\n\t"
    "v_and_b32 v21, 0xffff0000, v6\n\t"
    "v_lshlrev_b32 v22, 16, v7\n\t"
    "v_and_b32 v23, 0xffff0000, v7\n\t"
    "v_pk_mul_f32 v[16:17], v[16:17], s[80:81]\n\t"
    "v_pk_mul_f32 v[18:19], v[18:19], s[80:81]\n\t"
    "v_pk_mul_f32 v[20:21], v[20:21], s[80:81]\n\t"
    "v_pk_mul_f32 v[22:23], v[22:23], s[80:81]\n\t"
    "v_cvt_pk_bf16_f32 v4, v16, v17\n\t"
    "v_cvt_pk_bf16_f32 v5, v18, v19\n\t"
    "v_cvt_pk_bf16_f32 v6, v20, v21\n\t"
    "v_cvt_pk_bf16_f32 v7, v22, v23\n\t"
    "v_accvgpr_write_b32 a4, v4\n\t"
    "v_accvgpr_write_b32 a5, v5\n\t"
    "v_accvgpr_write_b32 a6, v6\n\t"
    "v_accvgpr_write_b32 a7, v7\n\t"
    "s_waitcnt lgkmcnt(1)\n\t"
    "v_lshlrev_b32 v16, 16, v8\n\t"
    "v_and_b32 v17, 0xffff0000, v8\n\t"
    "v_lshlrev_b32 v18, 16, v9\n\t"
    "v_and_b32 v19, 0xffff0000, v9\n\t"
    "v_lshlrev_b32 v20, 16, v10\n\t"
    "v_and_b32 v21, 0xffff0000, v10\n\t"
    "v_lshlrev_b32 v22, 16, v11\n\t"
    "v_and_b32 v23, 0xffff0000, v11\n\t"
    "v_pk_mul_f32 v[16:17], v[16:17], s[80:81]\n\t"
    "v_pk_mul_f32 v[18:19], v[18:19], s[80:81]\n\t"
    "v_pk_mul_f32 v[20:21], v[20:21], s[80:81]\n\t"
    "v_pk_mul_f32 v[22:23], v[22:23], s[80:81]\n\t"
    "v_cvt_pk_bf16_f32 v8, v16, v17\n\t"
    "v_cvt_pk_bf16_f32 v9, v18, v19\n\t"
    "v_cvt_pk_bf16_f32 v10, v20, v21\n\t"
    "v_cvt_pk_bf16_f32 v11, v22, v23\n\t"
    "v_accvgpr_write_b32 a8, v8\n\t"
    "v_accvgpr_write_b32 a9, v9\n\t"
    "v_accvgpr_write_b32 a10, v10\n\t"
    "v_accvgpr_write_b32 a11, v11\n\t"
    "s_waitcnt lgkmcnt(0)\n\t"
    "v_lshlrev_b32 v16, 16, v12\n\t"
    "v_and_b32 v17, 0xffff0000, v12\n\t"
    "v_lshlrev_b32 v18, 16, v13\n\t"
    "v_and_b32 v19, 0xffff0000, v13\n\t"
    "v_lshlrev_b32 v20, 16, v14\n\t"
    "v_and_b32 v21, 0xffff0000, v14\n\t"
    "v_lshlrev_b32 v22, 16, v15\n\t"
    "v_and_b32 v23, 0xffff0000, v15\n\t"
    "v_pk_mul_f32 v[16:17], v[16:17], s[80:81]\n\t"
    "v_pk_mul_f32 v[18:19], v[18:19], s[80:81]\n\t"
    "v_pk_mul_f32 v[20:21], v[20:21], s[80:81]\n\t"
    "v_pk_mul_f32 v[22:23], v[22:23], s[80:81]\n\t"
    "v_cvt_pk_bf16_f32 v12, v16, v17\n\t"
    "v_cvt_pk_bf16_f32 v13, v18, v19\n\t"
    "v_cvt_pk_bf16_f32 v14, v20, v21\n\t"
    "v_cvt_pk_bf16_f32 v15, v22, v23\n\t"
    "v_accvgpr_write_b32 a12, v12\n\t"
    "v_accvgpr_write_b32 a13, v13\n\t"
    "v_accvgpr_write_b32 a14, v14\n\t"
    "v_accvgpr_write_b32 a15, v15\n\t"
    "s_cmp_lt_u32 s74, 2\n\t"
    "s_cbranch_scc1 L_qw0_%=\n\t"
    "ds_write_b128 v252, v[0:3] offset:8192\n\t"
    "ds_write_b128 v252, v[4:7] offset:8704\n\t"
    "ds_write_b128 v252, v[8:11] offset:9216\n\t"
    "ds_write_b128 v252, v[12:15] offset:9728\n\t"
    "L_qw0_%=:\n\t"
    "s_waitcnt vmcnt(8) lgkmcnt(0)\n\t"
    "ds_read_b128 v[32:35], v252 offset:12288\n\t"
    "ds_read_b128 v[36:39], v252 offset:12800\n\t"
    "ds_read_b128 v[40:43], v252 offset:13312\n\t"
    "ds_read_b128 v[44:47], v252 offset:13824\n\t"
    "s_waitcnt lgkmcnt(3)\n\t"
    "v_lshlrev_b32 v48, 16, v32\n\t"
    "v_and_b32 v49, 0xffff0000, v32\n\t"
    "v_lshlrev_b32 v50, 16, v33\n\t"
    "v_and_b32 v51, 0xffff0000, v33\n\t"
    "v_lshlrev_b32 v52, 16, v34\n\t"
    "v_and_b32 v53, 0xffff0000, v34\n\t"
    "v_lshlrev_b32 v54, 16, v35\n\t"
    "v_and_b32 v55, 0xffff0000, v35\n\t"
    "v_pk_mul_f32 v[48:49], v[48:49], s[80:81]\n\t"
    "v_pk_mul_f32 v[50:51], v[50:51], s[80:81]\n\t"
    "v_pk_mul_f32 v[52:53], v[52:53], s[80:81]\n\t"
    "v_pk_mul_f32 v[54:55], v[54:55], s[80:81]\n\t"
    "v_mfma_f32_16x16x32_bf16 v[128:131], a[64:67], a[0:3], v[160:163]\n\t"
    "v_cvt_pk_bf16_f32 v32, v48, v49\n\t"
    "v_cvt_pk_bf16_f32 v33, v50, v51\n\t"
    "v_mfma_f32_16x16x32_bf16 v[128:131], a[68:71], a[4:7], v[128:131]\n\t"
    "v_cvt_pk_bf16_f32 v34, v52, v53\n\t"
    "v_cvt_pk_bf16_f32 v35, v54, v55\n\t"
    "v_accvgpr_write_b32 a16, v32\n\t"
    "v_accvgpr_write_b32 a17, v33\n\t"
    "v_accvgpr_write_b32 a18, v34\n\t"
    "v_accvgpr_write_b32 a19, v35\n\t"
    "s_waitcnt lgkmcnt(2)\n\t"
    "v_lshlrev_b32 v48, 16, v36\n\t"
    "v_and_b32 v49, 0xffff0000, v36\n\t"
    "v_lshlrev_b32 v50, 16, v37\n\t"
    "v_and_b32 v51, 0xffff0000, v37\n\t"
    "v_lshlrev_b32 v52, 16, v38\n\t"
    "v_and_b32 v53, 0xffff0000, v38\n\t"
    "v_lshlrev_b32 v54, 16, v39\n\t"
    "v_and_b32 v55, 0xffff0000, v39\n\t"
    "v_pk_mul_f32 v[48:49], v[48:49], s[80:81]\n\t"
    "v_pk_mul_f32 v[50:51], v[50:51], s[80:81]\n\t"
    "v_pk_mul_f32 v[52:53], v[52:53], s[80:81]\n\t"
    "v_pk_mul_f32 v[54:55], v[54:55], s[80:81]\n\t"
    "v_mfma_f32_16x16x32_bf16 v[128:131], a[72:75], a[8:11], v[128:131]\n\t"
    "v_cvt_pk_bf16_f32 v36, v48, v49\n\t"
    "v_cvt_pk_bf16_f32 v37, v50, v51\n\t"
    "v_mfma_f32_16x16x32_bf16 v[128:131], a[76:79], a[12:15], v[128:131]\n\t"
    "v_cvt_pk_bf16_f32 v38, v52, v53\n\t"
    "v_cvt_pk_bf16_f32 v39, v54, v55\n\t"
    "v_accvgpr_write_b32 a20, v36\n\t"
    "v_accvgpr_write_b32 a21, v37\n\t"
    "v_accvgpr_write_b32 a22, v38\n\t"
    "v_accvgpr_write_b32 a23, v39\n\t"
    "s_waitcnt lgkmcnt(1)\n\t"
    "v_lshlrev_b32 v48, 16, v40\n\t"
    "v_and_b32 v49, 0xffff0000, v40\n\t"
    "v_lshlrev_b32 v50, 16, v41\n\t"
    "v_and_b32 v51, 0xffff0000, v41\n\t"
    "v_lshlrev_b32 v52, 16, v42\n\t"
    "v_and_b32 v53, 0xffff0000, v42\n\t"
    "v_lshlrev_b32 v54, 16, v43\n\t"
    "v_and_b32 v55, 0xffff0000, v43\n\t"
    "v_pk_mul_f32 v[48:49], v[48:49], s[80:81]\n\t"
    "v_pk_mul_f32 v[50:51], v[50:51], s[80:81]\n\t"
    "v_pk_mul_f32 v[52:53], v[52:53], s[80:81]\n\t"
    "v_pk_mul_f32 v[54:55], v[54:55], s[80:81]\n\t"
    "v_mfma_f32_16x16x32_bf16 v[132:135], a[80:83], a[0:3], v[164:167]\n\t"
    "v_cvt_pk_bf16_f32 v40, v48, v49\n\t"
    "v_cvt_pk_bf16_f32 v41, v50, v51\n\t"
    "v_mfma_f32_16x16x32_bf16 v[132:135], a[84:87], a[4:7], v[132:135]\n\t"
    "v_cvt_pk_bf16_f32 v42, v52, v53\n\t"
    "v_cvt_pk_bf16_f32 v43, v54, v55\n\t"
    "v_accvgpr_write_b32 a24, v40\n\t"
    "v_accvgpr_write_b32 a25, v41\n\t"
    "v_accvgpr_write_b32 a26, v42\n\t"
    "v_accvgpr_write_b32 a27, v43\n\t"
    "s_waitcnt lgkmcnt(0)\n\t"
    "v_lshlrev_b32 v48, 16, v44\n\t"
    "v_and_b32 v49, 0xffff0000, v44\n\t"
    "v_lshlrev_b32 v50, 16, v45\n\t"
    "v_and_b32 v51, 0xffff0000, v45\n\t"
    "v_lshlrev_b32 v52, 16, v46\n\t"
    "v_and_b32 v53, 0xffff0000, v46\n\t"
    "v_lshlrev_b32 v54, 16, v47\n\t"
    "v_and_b32 v55, 0xffff0000, v47\n\t"
    "v_pk_mul_f32 v[48:49], v[48:49], s[80:81]\n\t"
    "v_pk_mul_f32 v[50:51], v[50:51], s[80:81]\n\t"
    "v_pk_mul_f32 v[52:53], v[52:53], s[80:81]\n\t"
    "v_pk_mul_f32 v[54:55], v[54:55], s[80:81]\n\t"
    "v_mfma_f32_16x16x32_bf16 v[132:135], a[88:91], a[8:11], v[132:135]\n\t"
    "v_cvt_pk_bf16_f32 v44, v48, v49\n\t"
    "v_cvt_pk_bf16_f32 v45, v50, v51\n\t"
    "v_mfma_f32_16x16x32_bf16 v[132:135], a[92:95], a[12:15], v[132:135]\n\t"
    "v_cvt_pk_bf16_f32 v46, v52, v53\n\t"
    "v_cvt_pk_bf16_f32 v47, v54, v55\n\t"
    "v_accvgpr_write_b32 a28, v44\n\t"
    "v_accvgpr_write_b32 a29, v45\n\t"
    "v_accvgpr_write_b32 a30, v46\n\t"
    "v_accvgpr_write_b32 a31, v47\n\t"
    "s_cmp_lt_u32 s74, 2\n\t"
    "s_cbranch_scc1 L_qw1_%=\n\t"
    "ds_write_b128 v252, v[32:35] offset:12288\n\t"
    "ds_write_b128 v252, v[36:39] offset:12800\n\t"
    "ds_write_b128 v252, v[40:43] offset:13312\n\t"
    "ds_write_b128 v252, v[44:47] offset:13824\n\t"
    "L_qw1_%=:\n\t"
    "s_add_u32 m0, s30, 0\n\t"
    "s_mov_b32 s56, 8192\n\t"
    "buffer_load_dwordx4 v235, s[36:39], s56 offen offset:0 lds\n\t"
    "buffer_load_dwordx4 v236, s[36:39], s56 offen offset:1024 lds\n\t"
    "buffer_load_dwordx4 v235, s[40:43], s56 offen offset:2048 lds\n\t"
    "buffer_load_dwordx4 v236, s[40:43], s56 offen offset:3072 lds\n\t"
    "s_waitcnt vmcnt(8) lgkmcnt(0)\n\t"
    "ds_read_b128 v[0:3], v252 offset:0\n\t"
    "ds_read_b128 v[4:7], v252 offset:512\n\t"
    "ds_read_b128 v[8:11], v252 offset:1024\n\t"
    "ds_read_b128 v[12:15], v252 offset:1536\n\t"
    "s_waitcnt lgkmcnt(3)\n\t"
    "v_lshlrev_b32 v16, 16, v0\n\t"
    "v_and_b32 v17, 0xffff0000, v0\n\t"
    "v_lshlrev_b32 v18, 16, v1\n\t"
    "v_and_b32 v19, 0xffff0000, v1\n\t"
    "v_lshlrev_b32 v20, 16, v2\n\t"
    "v_and_b32 v21, 0xffff0000, v2\n\t"
    "v_lshlrev_b32 v22, 16, v3\n\t"
    "v_and_b32 v23, 0xffff0000, v3\n\t"
    "v_pk_mul_f32 v[16:17], v[16:17], s[80:81]\n\t"
    "v_pk_mul_f32 v[18:19], v[18:19], s[80:81]\n\t"
    "v_pk_mul_f32 v[20:21], v[20:21], s[80:81]\n\t"
    "v_pk_mul_f32 v[22:23], v[22:23], s[80:81]\n\t"
    "v_mfma_f32_16x16x32_bf16 v[136:139], a[64:67], a[16:19], v[168:171]\n\t"
    "v_cvt_pk_bf16_f32 v0, v16, v17\n\t"
    "v_cvt_pk_bf16_f32 v1, v18, v19\n\t"
    "v_mfma_f32_16x16x32_bf16 v[136:139], a[68:71], a[20:23], v[136:139]\n\t"
    "v_cvt_pk_bf16_f32 v2, v20, v21\n\t"
    "v_cvt_pk_bf16_f32 v3, v22, v23\n\t"
    "v_accvgpr_write_b32 a32, v0\n\t"
    "v_accvgpr_write_b32 a33, v1\n\t"
    "v_accvgpr_write_b32 a34, v2\n\t"
    "v_accvgpr_write_b32 a35, v3\n\t"
    "s_waitcnt lgkmcnt(2)\n\t"
    "v_lshlrev_b32 v16, 16, v4\n\t"
    "v_and_b32 v17, 0xffff0000, v4\n\t"
    "v_lshlrev_b32 v18, 16, v5\n\t"
    "v_and_b32 v19, 0xffff0000, v5\n\t"
    "v_lshlrev_b32 v20, 16, v6\n\t"
    "v_and_b32 v21, 0xffff0000, v6\n\t"
    "v_lshlrev_b32 v22, 16, v7\n\t"
    "v_and_b32 v23, 0xffff0000, v7\n\t"
    "v_pk_mul_f32 v[16:17], v[16:17], s[80:81]\n\t"
    "v_pk_mul_f32 v[18:19], v[18:19], s[80:81]\n\t"
    "v_pk_mul_f32 v[20:21], v[20:21], s[80:81]\n\t"
    "v_pk_mul_f32 v[22:23], v[22:23], s[80:81]\n\t"
    "v_mfma_f32_16x16x32_bf16 v[136:139], a[72:75], a[24:27], v[136:139]\n\t"
    "v_cvt_pk_bf16_f32 v4, v16, v17\n\t"
    "v_cvt_pk_bf16_f32 v5, v18, v19\n\t"
    "v_mfma_f32_16x16x32_bf16 v[136:139], a[76:79], a[28:31], v[136:139]\n\t"
    "v_cvt_pk_bf16_f32 v6, v20, v21\n\t"
    "v_cvt_pk_bf16_f32 v7, v22, v23\n\t"
    "v_accvgpr_write_b32 a36, v4\n\t"
    "v_accvgpr_write_b32 a37, v5\n\t"
    "v_accvgpr_write_b32 a38, v6\n\t"
    "v_accvgpr_write_b32 a39, v7\n\t"
    "s_waitcnt lgkmcnt(1)\n\t"
    "v_lshlrev_b32 v16, 16, v8\n\t"
    "v_and_b32 v17, 0xffff0000, v8\n\t"
    "v_lshlrev_b32 v18, 16, v9\n\t"
    "v_and_b32 v19, 0xffff0000, v9\n\t"
    "v_lshlrev_b32 v20, 16, v10\n\t"
    "v_and_b32 v21, 0xffff0000, v10\n\t"
    "v_lshlrev_b32 v22, 16, v11\n\t"
    "v_and_b32 v23, 0xffff0000, v11\n\t"
    "v_pk_mul_f32 v[16:17], v[16:17], s[80:81]\n\t"
    "v_pk_mul_f32 v[18:19], v[18:19], s[80:81]\n\t"
    "v_pk_mul_f32 v[20:21], v[20:21], s[80:81]\n\t"
    "v_pk_mul_f32 v[22:23], v[22:23], s[80:81]\n\t"
    "v_mfma_f32_16x16x32_bf16 v[140:143], a[80:83], a[16:19], v[172:175]\n\t"
    "v_cvt_pk_bf16_f32 v8, v16, v17\n\t"
    "v_cvt_pk_bf16_f32 v9, v18, v19\n\t"
    "v_mfma_f32_16x16x32_bf16 v[140:143], a[84:87], a[20:23], v[140:143]\n\t"
    "v_cvt_pk_bf16_f32 v10, v20, v21\n\t"
    "v_cvt_pk_bf16_f32 v11, v22, v23\n\t"
    "v_accvgpr_write_b32 a40, v8\n\t"
    "v_accvgpr_write_b32 a41, v9\n\t"
    "v_accvgpr_write_b32 a42, v10\n\t"
    "v_accvgpr_write_b32 a43, v11\n\t"
    "s_waitcnt lgkmcnt(0)\n\t"
    "v_lshlrev_b32 v16, 16, v12\n\t"
    "v_and_b32 v17, 0xffff0000, v12\n\t"
    "v_lshlrev_b32 v18, 16, v13\n\t"
    "v_and_b32 v19, 0xffff0000, v13\n\t"
    "v_lshlrev_b32 v20, 16, v14\n\t"
    "v_and_b32 v21, 0xffff0000, v14\n\t"
    "v_lshlrev_b32 v22, 16, v15\n\t"
    "v_and_b32 v23, 0xffff0000, v15\n\t"
    "v_pk_mul_f32 v[16:17], v[16:17], s[80:81]\n\t"
    "v_pk_mul_f32 v[18:19], v[18:19], s[80:81]\n\t"
    "v_pk_mul_f32 v[20:21], v[20:21], s[80:81]\n\t"
    "v_pk_mul_f32 v[22:23], v[22:23], s[80:81]\n\t"
    "v_mfma_f32_16x16x32_bf16 v[140:143], a[88:91], a[24:27], v[140:143]\n\t"
    "v_cvt_pk_bf16_f32 v12, v16, v17\n\t"
    "v_cvt_pk_bf16_f32 v13, v18, v19\n\t"
    "v_mfma_f32_16x16x32_bf16 v[140:143], a[92:95], a[28:31], v[140:143]\n\t"
    "v_cvt_pk_bf16_f32 v14, v20, v21\n\t"
    "v_cvt_pk_bf16_f32 v15, v22, v23\n\t"
    "v_accvgpr_write_b32 a44, v12\n\t"
    "v_accvgpr_write_b32 a45, v13\n\t"
    "v_accvgpr_write_b32 a46, v14\n\t"
    "v_accvgpr_write_b32 a47, v15\n\t"
    "s_cmp_lt_u32 s74, 2\n\t"
    "s_cbranch_scc1 L_qw2_%=\n\t"
    "ds_write_b128 v252, v[0:3] offset:0\n\t"
    "ds_write_b128 v252, v[4:7] offset:512\n\t"
    "ds_write_b128 v252, v[8:11] offset:1024\n\t"
    "ds_write_b128 v252, v[12:15] offset:1536\n\t"
    "L_qw2_%=:\n\t"
    "s_add_u32 m0, s30, 8192\n\t"
    "s_mov_b32 s56, 16384\n\t"
    "buffer_load_dwordx4 v235, s[36:39], s56 offen offset:0 lds\n\t"
    "buffer_load_dwordx4 v236, s[36:39], s56 offen offset:1024 lds\n\t"
    "buffer_load_dwordx4 v235, s[40:43], s56 offen offset:2048 lds\n\t"
    "buffer_load_dwordx4 v236, s[40:43], s56 offen offset:3072 lds\n\t"
    "v_max3_f32 v242, v128, v129, v130\n\t"
    "v_max3_f32 v243, v131, v132, v133\n\t"
    "v_max3_f32 v244, v136, v137, v138\n\t"
    "v_max3_f32 v245, v139, v140, v141\n\t"
    "v_max3_f32 v242, v242, v134, v135\n\t"
    "v_max3_f32 v244, v244, v142, v143\n\t"
    "v_max_f32 v242, v242, v243\n\t"
    "v_max_f32 v244, v244, v245\n\t"
    "v_mov_b32 v243, v242\n\t"
    "v_mov_b32 v245, v244\n\t"
    "s_nop 0\n\t"
    "v_permlane32_swap_b32 v242, v243\n\t"
    "v_permlane32_swap_b32 v244, v245\n\t"
    "v_max_f32 v242, v242, v243\n\t"
    "v_max_f32 v244, v244, v245\n\t"
    "v_mov_b32 v243, v242\n\t"
    "v_mov_b32 v245, v244\n\t"
    "s_nop 0\n\t"
    "v_permlane16_swap_b32 v242, v243\n\t"
    "v_permlane16_swap_b32 v244, v245\n\t"
    "v_max_f32 v242, v242, v243\n\t"
    "v_max_f32 v244, v244, v245\n\t"
    "v_sub_f32 v242, 0, v242\n\t"
    "v_sub_f32 v244, 0, v244\n\t"
    "v_mov_b32 v160, v242\n\t"
    "v_mov_b32 v161, v242\n\t"
    "v_mov_b32 v162, v242\n\t"
    "v_mov_b32 v163, v242\n\t"
    "v_mov_b32 v164, v242\n\t"
    "v_mov_b32 v165, v242\n\t"
    "v_mov_b32 v166, v242\n\t"
    "v_mov_b32 v167, v242\n\t"
    "v_mov_b32 v168, v244\n\t"
    "v_mov_b32 v169, v244\n\t"
    "v_mov_b32 v170, v244\n\t"
    "v_mov_b32 v171, v244\n\t"
    "v_mov_b32 v172, v244\n\t"
    "v_mov_b32 v173, v244\n\t"
    "v_mov_b32 v174, v244\n\t"
    "v_mov_b32 v175, v244\n\t"
    "v_add_f32 v128, v242, v128\n\t"
    "v_add_f32 v129, v242, v129\n\t"
    "v_add_f32 v130, v242, v130\n\t"
    "v_add_f32 v131, v242, v131\n\t"
    "v_add_f32 v132, v242, v132\n\t"
    "v_add_f32 v133, v242, v133\n\t"
    "v_add_f32 v134, v242, v134\n\t"
    "v_add_f32 v135, v242, v135\n\t"
    "v_add_f32 v136, v244, v136\n\t"
    "v_add_f32 v137, v244, v137\n\t"
    "v_add_f32 v138, v244, v138\n\t"
    "v_add_f32 v139, v244, v139\n\t"
    "v_add_f32 v140, v244, v140\n\t"
    "v_add_f32 v141, v244, v141\n\t"
    "v_add_f32 v142, v244, v142\n\t"
    "v_add_f32 v143, v244, v143\n\t"
    "v_exp_f32 v208, v128\n\t"
    "v_exp_f32 v209, v129\n\t"
    "s_nop 0\n\t"
    "v_cvt_pk_bf16_f32 v192, v208, v209\n\t"
    "v_exp_f32 v210, v130\n\t"
    "v_exp_f32 v211, v131\n\t"
    "s_nop 0\n\t"
    "v_cvt_pk_bf16_f32 v193, v210, v211\n\t"
    "v_exp_f32 v208, v132\n\t"
    "v_exp_f32 v209, v133\n\t"
    "s_nop 0\n\t"
    "v_cvt_pk_bf16_f32 v194, v208, v209\n\t"
    "v_exp_f32 v210, v134\n\t"
    "v_exp_f32 v211, v135\n\t"
    "s_nop 0\n\t"
    "v_cvt_pk_bf16_f32 v195, v210, v211\n\t"
    "v_exp_f32 v208, v136\n\t"
    "v_exp_f32 v209, v137\n\t"
    "s_nop 0\n\t"
    "v_cvt_pk_bf16_f32 v196, v208, v209\n\t"
    "v_exp_f32 v210, v138\n\t"
    "v_exp_f32 v211, v139\n\t"
    "s_nop 0\n\t"
    "v_cvt_pk_bf16_f32 v197, v210, v211\n\t"
    "v_exp_f32 v208, v140\n\t"
    "s_waitcnt vmcnt(8) lgkmcnt(0)\n\t"
    "ds_read_b128 v[32:35], v252 offset:4096\n\t"
    "ds_read_b128 v[36:39], v252 offset:4608\n\t"
    "ds_read_b128 v[40:43], v252 offset:5120\n\t"
    "ds_read_b128 v[44:47], v252 offset:5632\n\t"
    "v_mfma_f32_32x32x16_bf16 v[0:15], v[228:231], v[228:231], 0\n\t"
    "v_mfma_f32_32x32x16_bf16 v[16:31], v[228:231], v[228:231], 0\n\t"
    "s_waitcnt lgkmcnt(3)\n\t"
    "v_lshlrev_b32 v48, 16, v32\n\t"
    "v_and_b32 v49, 0xffff0000, v32\n\t"
    "v_lshlrev_b32 v50, 16, v33\n\t"
    "v_and_b32 v51, 0xffff0000, v33\n\t"
    "v_lshlrev_b32 v52, 16, v34\n\t"
    "v_and_b32 v53, 0xffff0000, v34\n\t"
    "v_lshlrev_b32 v54, 16, v35\n\t"
    "v_and_b32 v55, 0xffff0000, v35\n\t"
    "v_pk_mul_f32 v[48:49], v[48:49], s[80:81]\n\t"
    "v_pk_mul_f32 v[50:51], v[50:51], s[80:81]\n\t"
    "v_pk_mul_f32 v[52:53], v[52:53], s[80:81]\n\t"
    "v_pk_mul_f32 v[54:55], v[54:55], s[80:81]\n\t"
    "v_mfma_f32_16x16x32_bf16 v[144:147], a[64:67], a[32:35], v[176:179]\n\t"
    "v_cvt_pk_bf16_f32 v32, v48, v49\n\t"
    "v_cvt_pk_bf16_f32 v33, v50, v51\n\t"
    "v_mfma_f32_16x16x32_bf16 v[144:147], a[68:71], a[36:39], v[144:147]\n\t"
    "v_cvt_pk_bf16_f32 v34, v52, v53\n\t"
    "v_cvt_pk_bf16_f32 v35, v54, v55\n\t"
    "v_accvgpr_write_b32 a48, v32\n\t"
    "v_accvgpr_write_b32 a49, v33\n\t"
    "v_accvgpr_write_b32 a50, v34\n\t"
    "v_accvgpr_write_b32 a51, v35\n\t"
    "s_waitcnt lgkmcnt(2)\n\t"
    "v_lshlrev_b32 v48, 16, v36\n\t"
    "v_and_b32 v49, 0xffff0000, v36\n\t"
    "v_lshlrev_b32 v50, 16, v37\n\t"
    "v_and_b32 v51, 0xffff0000, v37\n\t"
    "v_lshlrev_b32 v52, 16, v38\n\t"
    "v_and_b32 v53, 0xffff0000, v38\n\t"
    "v_lshlrev_b32 v54, 16, v39\n\t"
    "v_and_b32 v55, 0xffff0000, v39\n\t"
    "v_pk_mul_f32 v[48:49], v[48:49], s[80:81]\n\t"
    "v_pk_mul_f32 v[50:51], v[50:51], s[80:81]\n\t"
    "v_pk_mul_f32 v[52:53], v[52:53], s[80:81]\n\t"
    "v_pk_mul_f32 v[54:55], v[54:55], s[80:81]\n\t"
    "v_mfma_f32_16x16x32_bf16 v[144:147], a[72:75], a[40:43], v[144:147]\n\t"
    "v_cvt_pk_bf16_f32 v36, v48, v49\n\t"
    "v_cvt_pk_bf16_f32 v37, v50, v51\n\t"
    "v_mfma_f32_16x16x32_bf16 v[144:147], a[76:79], a[44:47], v[144:147]\n\t"
    "v_cvt_pk_bf16_f32 v38, v52, v53\n\t"
    "v_cvt_pk_bf16_f32 v39, v54, v55\n\t"
    "v_accvgpr_write_b32 a52, v36\n\t"
    "v_accvgpr_write_b32 a53, v37\n\t"
    "v_accvgpr_write_b32 a54, v38\n\t"
    "v_accvgpr_write_b32 a55, v39\n\t"
    "s_waitcnt lgkmcnt(1)\n\t"
    "v_lshlrev_b32 v48, 16, v40\n\t"
    "v_and_b32 v49, 0xffff0000, v40\n\t"
    "v_lshlrev_b32 v50, 16, v41\n\t"
    "v_and_b32 v51, 0xffff0000, v41\n\t"
    "v_lshlrev_b32 v52, 16, v42\n\t"
    "v_and_b32 v53, 0xffff0000, v42\n\t"
    "v_lshlrev_b32 v54, 16, v43\n\t"
    "v_and_b32 v55, 0xffff0000, v43\n\t"
    "v_pk_mul_f32 v[48:49], v[48:49], s[80:81]\n\t"
    "v_pk_mul_f32 v[50:51], v[50:51], s[80:81]\n\t"
    "v_pk_mul_f32 v[52:53], v[52:53], s[80:81]\n\t"
    "v_pk_mul_f32 v[54:55], v[54:55], s[80:81]\n\t"
    "v_mfma_f32_16x16x32_bf16 v[148:151], a[80:83], a[32:35], v[180:183]\n\t"
    "v_cvt_pk_bf16_f32 v40, v48, v49\n\t"
    "v_cvt_pk_bf16_f32 v41, v50, v51\n\t"
    "v_mfma_f32_16x16x32_bf16 v[148:151], a[84:87], a[36:39], v[148:151]\n\t"
    "v_cvt_pk_bf16_f32 v42, v52, v53\n\t"
    "v_cvt_pk_bf16_f32 v43, v54, v55\n\t"
    "v_accvgpr_write_b32 a56, v40\n\t"
    "v_accvgpr_write_b32 a57, v41\n\t"
    "v_accvgpr_write_b32 a58, v42\n\t"
    "v_accvgpr_write_b32 a59, v43\n\t"
    "s_waitcnt lgkmcnt(0)\n\t"
    "v_lshlrev_b32 v48, 16, v44\n\t"
    "v_and_b32 v49, 0xffff0000, v44\n\t"
    "v_lshlrev_b32 v50, 16, v45\n\t"
    "v_and_b32 v51, 0xffff0000, v45\n\t"
    "v_lshlrev_b32 v52, 16, v46\n\t"
    "v_and_b32 v53, 0xffff0000, v46\n\t"
    "v_lshlrev_b32 v54, 16, v47\n\t"
    "v_and_b32 v55, 0xffff0000, v47\n\t"
    "v_pk_mul_f32 v[48:49], v[48:49], s[80:81]\n\t"
    "v_pk_mul_f32 v[50:51], v[50:51], s[80:81]\n\t"
    "v_pk_mul_f32 v[52:53], v[52:53], s[80:81]\n\t"
    "v_pk_mul_f32 v[54:55], v[54:55], s[80:81]\n\t"
    "v_mfma_f32_16x16x32_bf16 v[148:151], a[88:91], a[40:43], v[148:151]\n\t"
    "v_cvt_pk_bf16_f32 v44, v48, v49\n\t"
    "v_cvt_pk_bf16_f32 v45, v50, v51\n\t"
    "v_mfma_f32_16x16x32_bf16 v[148:151], a[92:95], a[44:47], v[148:151]\n\t"
    "v_cvt_pk_bf16_f32 v46, v52, v53\n\t"
    "v_cvt_pk_bf16_f32 v47, v54, v55\n\t"
    "v_accvgpr_write_b32 a60, v44\n\t"
    "v_accvgpr_write_b32 a61, v45\n\t"
    "v_accvgpr_write_b32 a62, v46\n\t"
    "v_accvgpr_write_b32 a63, v47\n\t"
    "v_mfma_f32_16x16x32_bf16 v[152:155], a[64:67], a[48:51], v[184:187]\n\t"
    "v_mfma_f32_16x16x32_bf16 v[152:155], a[68:71], a[52:55], v[152:155]\n\t"
    "v_mfma_f32_16x16x32_bf16 v[152:155], a[72:75], a[56:59], v[152:155]\n\t"
    "v_mfma_f32_16x16x32_bf16 v[152:155], a[76:79], a[60:63], v[152:155]\n\t"
    "v_mfma_f32_16x16x32_bf16 v[156:159], a[80:83], a[48:51], v[188:191]\n\t"
    "v_mfma_f32_16x16x32_bf16 v[156:159], a[84:87], a[52:55], v[156:159]\n\t"
    "v_mfma_f32_16x16x32_bf16 v[156:159], a[88:91], a[56:59], v[156:159]\n\t"
    "v_mfma_f32_16x16x32_bf16 v[156:159], a[92:95], a[60:63], v[156:159]\n\t"
    "s_cmp_lt_u32 s74, 2\n\t"
    "s_cbranch_scc1 L_qw3_%=\n\t"
    "ds_write_b128 v252, v[32:35] offset:4096\n\t"
    "ds_write_b128 v252, v[36:39] offset:4608\n\t"
    "ds_write_b128 v252, v[40:43] offset:5120\n\t"
    "ds_write_b128 v252, v[44:47] offset:5632\n\t"
    "L_qw3_%=:\n\t"
    "s_add_u32 m0, s30, 16384\n\t"
    "s_mov_b32 s56, 24576\n\t"
    "buffer_load_dwordx4 v235, s[36:39], s56 offen offset:0 lds\n\t"
    "buffer_load_dwordx4 v236, s[36:39], s56 offen offset:1024 lds\n\t"
    "buffer_load_dwordx4 v235, s[40:43], s56 offen offset:2048 lds\n\t"
    "buffer_load_dwordx4 v236, s[40:43], s56 offen offset:3072 lds\n\t"
    "s_add_u32 s76, s30, 90112\n\t"
    "s_cmp_lt_u32 s74, 2\n\t"
    "s_cselect_b32 m0, s83, s76\n\t"
    "s_mov_b32 s57, 253952\n\t"
    "buffer_load_dwordx4 v235, s[44:47], s57 offen offset:0 lds\n\t"
    "buffer_load_dwordx4 v236, s[44:47], s57 offen offset:1024 lds\n\t"
    "buffer_load_dwordx4 v235, s[48:51], s57 offen offset:2048 lds\n\t"
    "buffer_load_dwordx4 v236, s[48:51], s57 offen offset:3072 lds\n\t"
    "s_mov_b32 s56, 32768\n\t"
    "s_mov_b32 s57, 245760\n\t"
    "s_add_u32 m0, s30, 0x6000\n\t"
    "v_bfi_b32 v232, v237, s56, v232\n\t"
    "s_add_u32 s52, s26, s82\n\t"
    "s_addc_u32 s53, s27, 0\n\t"
    "s_mov_b32 s54, 0x80000\n\t"
    "s_waitcnt lgkmcnt(0)\n\t"
    "v_mfma_f32_32x32x16_bf16 v[32:47], v[228:231], v[228:231], 0\n\t"
    "v_mfma_f32_32x32x16_bf16 v[48:63], v[228:231], v[228:231], 0\n\t"
    "v_mov_b32 v228, v242\n\t"
    "v_mov_b32 v229, v244\n\t"
    "v_max3_f32 v242, v144, v145, v146\n\t"
    "v_max3_f32 v243, v147, v148, v149\n\t"
    "v_max3_f32 v244, v152, v153, v154\n\t"
    "v_max3_f32 v245, v155, v156, v157\n\t"
    "v_max3_f32 v242, v242, v150, v151\n\t"
    "v_max3_f32 v244, v244, v158, v159\n\t"
    "v_max_f32 v242, v242, v243\n\t"
    "v_max_f32 v244, v244, v245\n\t"
    "v_mov_b32 v243, v242\n\t"
    "v_mov_b32 v245, v244\n\t"
    "s_nop 0\n\t"
    "v_permlane32_swap_b32 v242, v243\n\t"
    "v_permlane32_swap_b32 v244, v245\n\t"
    "v_max_f32 v242, v242, v243\n\t"
    "v_max_f32 v244, v244, v245\n\t"
    "v_mov_b32 v243, v242\n\t"
    "v_mov_b32 v245, v244\n\t"
    "s_nop 0\n\t"
    "v_permlane16_swap_b32 v242, v243\n\t"
    "v_permlane16_swap_b32 v244, v245\n\t"
    "v_max_f32 v242, v242, v243\n\t"
    "v_max_f32 v244, v244, v245\n\t"
    "v_sub_f32 v230, 0, v242\n\t"
    "v_sub_f32 v231, 0, v244\n\t"
    "ds_write_b128 v253, v[228:231]\n\t"
    "v_mov_b32 v176, v230\n\t"
    "v_mov_b32 v177, v230\n\t"
    "v_mov_b32 v178, v230\n\t"
    "v_mov_b32 v179, v230\n\t"
    "v_mov_b32 v180, v230\n\t"
    "v_mov_b32 v181, v230\n\t"
    "v_mov_b32 v182, v230\n\t"
    "v_mov_b32 v183, v230\n\t"
    "v_mov_b32 v184, v231\n\t"
    "v_mov_b32 v185, v231\n\t"
    "v_mov_b32 v186, v231\n\t"
    "v_mov_b32 v187, v231\n\t"
    "v_mov_b32 v188, v231\n\t"
    "v_mov_b32 v189, v231\n\t"
    "v_mov_b32 v190, v231\n\t"
    "v_mov_b32 v191, v231\n\t"
    "s_cmp_lg_u32 s61, 0\n\t"
    "s_cbranch_scc1 L_nomask1_%=\n\t"
    "s_call_b64 s[66:67], L_hdiag2_%=\n\t"
    "L_nomask1_%=:\n\t"
    "s_waitcnt vmcnt(12) lgkmcnt(0)\n\t"
    "s_barrier\n\t"
    "ds_read_b64_tr_b16 a[96:97], v233 offset:2048\n\t"
    "ds_read_b64_tr_b16 a[98:99], v233 offset:34816\n\t"
    "ds_read_b64_tr_b16 a[100:101], v234 offset:2048\n\t"
    "ds_read_b64_tr_b16 a[102:103], v234 offset:34816\n\t"
    "ds_read_b64_tr_b16 a[104:105], v233 offset:2560\n\t"
    "ds_read_b64_tr_b16 a[106:107], v233 offset:35328\n\t"
    "ds_read_b64_tr_b16 a[108:109], v234 offset:2560\n\t"
    "ds_read_b64_tr_b16 a[110:111], v234 offset:35328\n\t"
    "ds_read_b64 a[136:137], v254 offset:8\n\t"
    "ds_read_b64 a[138:139], v254\n\t"
    "buffer_load_dwordx4 v235, s[36:39], s56 offen offset:0 lds\n\t"
    "buffer_load_dwordx4 v236, s[36:39], s56 offen offset:1024 lds\n\t"
    "buffer_load_dwordx4 v235, s[40:43], s56 offen offset:2048 lds\n\t"
    "s_branch L_entry_%=\n\t"
    ".p2align 6\n\t"
    "L_loop_%=:\n\t"
    "v_mfma_f32_16x16x32_bf16 v[64:67], a[96:99], v[200:203], v[64:67]\n\t"
    "v_exp_f32 v209, v133\n\t"
    "v_mfma_f32_16x16x32_bf16 v[96:99], a[96:99], v[204:207], v[96:99]\n\t"
    "ds_read_b64_tr_b16 a[96:97], v233 offset:2048\n\t"
    "v_cvt_pk_bf16_f32 v194, v208, v209\n\t"
    "v_mfma_f32_16x16x32_bf16 v[68:71], a[100:103], v[200:203], v[68:71]\n\t"
    "v_exp_f32 v210, v134\n\t"
    "v_mfma_f32_16x16x32_bf16 v[100:103], a[100:103], v[204:207], v[100:103]\n\t"
    "buffer_load_dwordx4 v236, s[36:39], s56 offen offset:1024 lds\n\t"
    "v_mfma_f32_16x16x32_bf16 v[72:75], a[104:107], v[200:203], v[72:75]\n\t"
    "v_exp_f32 v211, v135\n\t"
    "v_mfma_f32_16x16x32_bf16 v[104:107], a[104:107], v[204:207], v[104:107]\n\t"
    "ds_read_b64_tr_b16 a[98:99], v233 offset:34816\n\t"
    "v_cvt_pk_bf16_f32 v195, v210, v211\n\t"
    "v_mfma_f32_16x16x32_bf16 v[76:79], a[108:111], v[200:203], v[76:79]\n\t"
    "v_exp_f32 v208, v136\n\t"
    "v_mfma_f32_16x16x32_bf16 v[108:111], a[108:111], v[204:207], v[108:111]\n\t"
    "ds_read_b64_tr_b16 a[100:101], v234 offset:2048\n\t"
    "ds_read_b64_tr_b16 a[102:103], v234 offset:34816\n\t"
    "v_mfma_f32_16x16x32_bf16 v[80:83], a[112:115], v[200:203], v[80:83]\n\t"
    "v_exp_f32 v209, v137\n\t"
    "v_mfma_f32_16x16x32_bf16 v[112:115], a[112:115], v[204:207], v[112:115]\n\t"
    "ds_read_b64_tr_b16 a[104:105], v233 offset:2560\n\t"
    "v_cvt_pk_bf16_f32 v196, v208, v209\n\t"
    "v_mfma_f32_16x16x32_bf16 v[84:87], a[116:119], v[200:203], v[84:87]\n\t"
    "v_exp_f32 v210, v138\n\t"
    "v_mfma_f32_16x16x32_bf16 v[116:119], a[116:119], v[204:207], v[116:119]\n\t"
    "buffer_load_dwordx4 v235, s[40:43], s56 offen offset:2048 lds\n\t"
    "v_mfma_f32_16x16x32_bf16 v[88:91], a[120:123], v[200:203], v[88:91]\n\t"
    "v_exp_f32 v211, v139\n\t"
    "v_mfma_f32_16x16x32_bf16 v[120:123], a[120:123], v[204:207], v[120:123]\n\t"
    "ds_read_b64_tr_b16 a[106:107], v233 offset:35328\n\t"
    "v_cvt_pk_bf16_f32 v197, v210, v211\n\t"
    "v_mfma_f32_16x16x32_bf16 v[92:95], a[124:127], v[200:203], v[92:95]\n\t"
    "v_exp_f32 v208, v140\n\t"
    "v_mfma_f32_16x16x32_bf16 v[124:127], a[124:127], v[204:207], v[124:127]\n\t"
    "ds_read_b64_tr_b16 a[108:109], v234 offset:2560\n\t"
    "ds_read_b64_tr_b16 a[110:111], v234 offset:35328\n\t"
    "v_mfma_f32_16x16x32_bf16 v[144:147], a[64:67], a[32:35], v[176:179]\n\t"
    "v_exp_f32 v209, v141\n\t"
    "v_mfma_f32_16x16x32_bf16 v[144:147], a[68:71], a[36:39], v[144:147]\n\t"
    "ds_read_b64_tr_b16 a[112:113], v233 offset:3072\n\t"
    "v_cvt_pk_bf16_f32 v198, v208, v209\n\t"
    "v_mfma_f32_16x16x32_bf16 v[144:147], a[72:75], a[40:43], v[144:147]\n\t"
    "v_exp_f32 v210, v142\n\t"
    "v_mfma_f32_16x16x32_bf16 v[144:147], a[76:79], a[44:47], v[144:147]\n\t"
    "buffer_load_dwordx4 v236, s[40:43], s56 offen offset:3072 lds\n\t"
    "v_mfma_f32_16x16x32_bf16 v[212:215], a[128:131], v[192:195], v[212:215]\n\t"
    "v_exp_f32 v211, v143\n\t"
    "v_mfma_f32_16x16x32_bf16 v[148:151], a[80:83], a[32:35], v[180:183]\n\t"
    "ds_read_b64_tr_b16 a[114:115], v233 offset:35840\n\t"
    "v_cvt_pk_bf16_f32 v199, v210, v211\n\t"
    "v_mfma_f32_16x16x32_bf16 v[148:151], a[84:87], a[36:39], v[148:151]\n\t"
    "ds_read_b64_tr_b16 a[116:117], v234 offset:3072\n\t"
    "ds_read_b64_tr_b16 a[118:119], v234 offset:35840\n\t"
    "v_mfma_f32_16x16x32_bf16 v[148:151], a[88:91], a[40:43], v[148:151]\n\t"
    "ds_read_b64_tr_b16 a[120:121], v233 offset:3584\n\t"
    "ds_read_b64_tr_b16 a[122:123], v233 offset:36352\n\t"
    "v_mfma_f32_16x16x32_bf16 v[148:151], a[92:95], a[44:47], v[148:151]\n\t"
    "v_exp_f32 v208, v144\n\t"
    "v_mfma_f32_16x16x32_bf16 v[216:219], a[128:131], v[196:199], v[216:219]\n\t"
    "ds_read_b64_tr_b16 a[124:125], v234 offset:3584\n\t"
    "ds_read_b64_tr_b16 a[126:127], v234 offset:36352\n\t"
    "v_mfma_f32_16x16x32_bf16 v[152:155], a[64:67], a[48:51], v[184:187]\n\t"
    "v_exp_f32 v209, v145\n\t"
    "s_add_u32 m0, m0, 0x10000\n\t"
    "v_mfma_f32_16x16x32_bf16 v[152:155], a[68:71], a[52:55], v[152:155]\n\t"
    "ds_read_b128 a[64:67], v232 offset:0\n\t"
    "v_cvt_pk_bf16_f32 v200, v208, v209\n\t"
    "v_mfma_f32_16x16x32_bf16 v[152:155], a[72:75], a[56:59], v[152:155]\n\t"
    "v_exp_f32 v210, v146\n\t"
    "v_mfma_f32_16x16x32_bf16 v[152:155], a[76:79], a[60:63], v[152:155]\n\t"
    "buffer_load_dwordx4 v235, s[44:47], s57 offen offset:0 lds\n\t"
    "v_mfma_f32_16x16x32_bf16 v[156:159], a[80:83], a[48:51], v[188:191]\n\t"
    "v_exp_f32 v211, v147\n\t"
    "v_add_f32 v241, v212, v216\n\t"
    "v_mfma_f32_16x16x32_bf16 v[156:159], a[84:87], a[52:55], v[156:159]\n\t"
    "ds_read_b128 a[68:71], v232 offset:512\n\t"
    "v_cvt_pk_bf16_f32 v201, v210, v211\n\t"
    "v_mfma_f32_16x16x32_bf16 v[156:159], a[88:91], a[56:59], v[156:159]\n\t"
    "v_exp_f32 v208, v148\n\t"
    "v_cmp_nge_f32 vcc, s64, v241\n\t"
    "v_mfma_f32_16x16x32_bf16 v[156:159], a[92:95], a[60:63], v[156:159]\n\t"
    "s_cbranch_vccnz L_stub3_%=\n\t"
    "L_ret3_%=:\n\t"
    "s_waitcnt lgkmcnt(2)\n\t"
    "v_mfma_f32_16x16x32_bf16 v[0:3], a[96:99], v[192:195], v[0:3]\n\t"
    "v_exp_f32 v209, v149\n\t"
    "v_mfma_f32_16x16x32_bf16 v[32:35], a[96:99], v[196:199], v[32:35]\n\t"
    "ds_read_b128 a[72:75], v232 offset:1024\n\t"
    "v_cvt_pk_bf16_f32 v202, v208, v209\n\t"
    "v_mfma_f32_16x16x32_bf16 v[4:7], a[100:103], v[192:195], v[4:7]\n\t"
    "v_exp_f32 v210, v150\n\t"
    "v_mfma_f32_16x16x32_bf16 v[36:39], a[100:103], v[196:199], v[36:39]\n\t"
    "buffer_load_dwordx4 v236, s[44:47], s57 offen offset:1024 lds\n\t"
    "v_mfma_f32_16x16x32_bf16 v[8:11], a[104:107], v[192:195], v[8:11]\n\t"
    "v_exp_f32 v211, v151\n\t"
    "v_mfma_f32_16x16x32_bf16 v[40:43], a[104:107], v[196:199], v[40:43]\n\t"
    "ds_read_b128 a[76:79], v232 offset:1536\n\t"
    "v_cvt_pk_bf16_f32 v203, v210, v211\n\t"
    "v_mfma_f32_16x16x32_bf16 v[12:15], a[108:111], v[192:195], v[12:15]\n\t"
    "v_exp_f32 v208, v152\n\t"
    "v_mfma_f32_16x16x32_bf16 v[44:47], a[108:111], v[196:199], v[44:47]\n\t"
    "ds_read_b128 a[80:83], v232 offset:32768\n\t"
    "ds_read_b128 a[84:87], v232 offset:33280\n\t"
    "v_mfma_f32_16x16x32_bf16 v[16:19], a[112:115], v[192:195], v[16:19]\n\t"
    "v_exp_f32 v209, v153\n\t"
    "v_mfma_f32_16x16x32_bf16 v[48:51], a[112:115], v[196:199], v[48:51]\n\t"
    "ds_read_b128 a[88:91], v232 offset:33792\n\t"
    "v_cvt_pk_bf16_f32 v204, v208, v209\n\t"
    "v_mfma_f32_16x16x32_bf16 v[20:23], a[116:119], v[192:195], v[20:23]\n\t"
    "v_exp_f32 v210, v154\n\t"
    "v_mfma_f32_16x16x32_bf16 v[52:55], a[116:119], v[196:199], v[52:55]\n\t"
    "buffer_load_dwordx4 v235, s[48:51], s57 offen offset:2048 lds\n\t"
    "v_mfma_f32_16x16x32_bf16 v[24:27], a[120:123], v[192:195], v[24:27]\n\t"
    "v_exp_f32 v211, v155\n\t"
    "v_mfma_f32_16x16x32_bf16 v[56:59], a[120:123], v[196:199], v[56:59]\n\t"
    "ds_read_b128 a[92:95], v232 offset:34304\n\t"
    "v_cvt_pk_bf16_f32 v205, v210, v211\n\t"
    "v_mfma_f32_16x16x32_bf16 v[28:31], a[124:127], v[192:195], v[28:31]\n\t"
    "v_exp_f32 v208, v156\n\t"
    "v_mfma_f32_16x16x32_bf16 v[60:63], a[124:127], v[196:199], v[60:63]\n\t"
    "s_and_b32 s59, s56, 0x6000\n\t"
    "s_add_u32 s56, s56, 0x2000\n\t"
    "s_waitcnt lgkmcnt(4)\n\t"
    "v_mfma_f32_16x16x32_bf16 v[128:131], a[64:67], a[0:3], v[160:163]\n\t"
    "v_exp_f32 v209, v157\n\t"
    "v_mfma_f32_16x16x32_bf16 v[128:131], a[68:71], a[4:7], v[128:131]\n\t"
    "v_cvt_pk_bf16_f32 v206, v208, v209\n\t"
    "v_bfi_b32 v232, v237, s56, v232\n\t"
    "v_mfma_f32_16x16x32_bf16 v[128:131], a[72:75], a[8:11], v[128:131]\n\t"
    "v_exp_f32 v210, v158\n\t"
    "v_mfma_f32_16x16x32_bf16 v[128:131], a[76:79], a[12:15], v[128:131]\n\t"
    "buffer_load_dwordx4 v236, s[48:51], s57 offen offset:3072 lds\n\t"
    "v_mfma_f32_16x16x32_bf16 v[220:223], a[128:131], v[200:203], v[220:223]\n\t"
    "v_exp_f32 v211, v159\n\t"
    "s_waitcnt vmcnt(16) lgkmcnt(0)\n\t"
    "v_mfma_f32_16x16x32_bf16 v[132:135], a[80:83], a[0:3], v[164:167]\n\t"
    "v_cvt_pk_bf16_f32 v207, v210, v211\n\t"
    "v_bfi_b32 v233, v237, s56, v233\n\t"
    "v_mfma_f32_16x16x32_bf16 v[132:135], a[84:87], a[4:7], v[132:135]\n\t"
    "s_barrier\n\t"
    "v_mfma_f32_16x16x32_bf16 v[132:135], a[88:91], a[8:11], v[132:135]\n\t"
    "s_sub_u32 s57, s57, 0x2000\n\t"
    "s_or_b32 m0, s59, s30\n\t"
    "v_mfma_f32_16x16x32_bf16 v[132:135], a[92:95], a[12:15], v[132:135]\n\t"
    "v_exp_f32 v208, v128\n\t"
    "v_mfma_f32_16x16x32_bf16 v[224:227], a[128:131], v[204:207], v[224:227]\n\t"
    "buffer_load_dwordx4 v235, s[36:39], s56 offen offset:0 lds\n\t"
    "v_mfma_f32_16x16x32_bf16 v[136:139], a[64:67], a[16:19], v[168:171]\n\t"
    "v_exp_f32 v209, v129\n\t"
    "v_mfma_f32_16x16x32_bf16 v[136:139], a[68:71], a[20:23], v[136:139]\n\t"
    "v_cvt_pk_bf16_f32 v192, v208, v209\n\t"
    "v_bfi_b32 v234, v237, s56, v234\n\t"
    "v_mfma_f32_16x16x32_bf16 v[136:139], a[72:75], a[24:27], v[136:139]\n\t"
    "v_exp_f32 v210, v130\n\t"
    "v_mfma_f32_16x16x32_bf16 v[136:139], a[76:79], a[28:31], v[136:139]\n\t"
    "s_cmp_eq_u32 s56, s58\n\t"
    "s_cselect_b32 s31, -1.0, s64\n\t"
    "v_mfma_f32_16x16x32_bf16 v[140:143], a[80:83], a[16:19], v[172:175]\n\t"
    "v_exp_f32 v211, v131\n\t"
    "v_mfma_f32_16x16x32_bf16 v[140:143], a[84:87], a[20:23], v[140:143]\n\t"
    "v_cvt_pk_bf16_f32 v193, v210, v211\n\t"
    "v_add_f32 v241, v220, v224\n\t"
    "v_mfma_f32_16x16x32_bf16 v[140:143], a[88:91], a[24:27], v[140:143]\n\t"
    "v_exp_f32 v208, v132\n\t"
    "v_cmp_nge_f32 vcc, s31, v241\n\t"
    "v_mfma_f32_16x16x32_bf16 v[140:143], a[92:95], a[28:31], v[140:143]\n\t"
    "s_cbranch_vccnz L_slow_%=\n\t"
    "v_mfma_f32_16x16x32_bf16 v[64:67], a[96:99], v[200:203], v[64:67]\n\t"
    "v_exp_f32 v209, v133\n\t"
    "v_mfma_f32_16x16x32_bf16 v[96:99], a[96:99], v[204:207], v[96:99]\n\t"
    "ds_read_b64_tr_b16 a[96:97], v233 offset:2048\n\t"
    "v_cvt_pk_bf16_f32 v194, v208, v209\n\t"
    "v_mfma_f32_16x16x32_bf16 v[68:71], a[100:103], v[200:203], v[68:71]\n\t"
    "v_exp_f32 v210, v134\n\t"
    "v_mfma_f32_16x16x32_bf16 v[100:103], a[100:103], v[204:207], v[100:103]\n\t"
    "buffer_load_dwordx4 v236, s[36:39], s56 offen offset:1024 lds\n\t"
    "v_mfma_f32_16x16x32_bf16 v[72:75], a[104:107], v[200:203], v[72:75]\n\t"
    "v_exp_f32 v211, v135\n\t"
    "v_mfma_f32_16x16x32_bf16 v[104:107], a[104:107], v[204:207], v[104:107]\n\t"
    "ds_read_b64_tr_b16 a[98:99], v233 offset:34816\n\t"
    "v_cvt_pk_bf16_f32 v195, v210, v211\n\t"
    "v_mfma_f32_16x16x32_bf16 v[76:79], a[108:111], v[200:203], v[76:79]\n\t"
    "v_exp_f32 v208, v136\n\t"
    "v_mfma_f32_16x16x32_bf16 v[108:111], a[108:111], v[204:207], v[108:111]\n\t"
    "ds_read_b64_tr_b16 a[100:101], v234 offset:2048\n\t"
    "ds_read_b64_tr_b16 a[102:103], v234 offset:34816\n\t"
    "v_mfma_f32_16x16x32_bf16 v[80:83], a[112:115], v[200:203], v[80:83]\n\t"
    "v_exp_f32 v209, v137\n\t"
    "v_mfma_f32_16x16x32_bf16 v[112:115], a[112:115], v[204:207], v[112:115]\n\t"
    "ds_read_b64_tr_b16 a[104:105], v233 offset:2560\n\t"
    "v_cvt_pk_bf16_f32 v196, v208, v209\n\t"
    "v_mfma_f32_16x16x32_bf16 v[84:87], a[116:119], v[200:203], v[84:87]\n\t"
    "v_exp_f32 v210, v138\n\t"
    "v_mfma_f32_16x16x32_bf16 v[116:119], a[116:119], v[204:207], v[116:119]\n\t"
    "buffer_load_dwordx4 v235, s[40:43], s56 offen offset:2048 lds\n\t"
    "v_mfma_f32_16x16x32_bf16 v[88:91], a[120:123], v[200:203], v[88:91]\n\t"
    "v_exp_f32 v211, v139\n\t"
    "v_mfma_f32_16x16x32_bf16 v[120:123], a[120:123], v[204:207], v[120:123]\n\t"
    "ds_read_b64_tr_b16 a[106:107], v233 offset:35328\n\t"
    "v_cvt_pk_bf16_f32 v197, v210, v211\n\t"
    "v_mfma_f32_16x16x32_bf16 v[92:95], a[124:127], v[200:203], v[92:95]\n\t"
    "v_exp_f32 v208, v140\n\t"
    "v_mfma_f32_16x16x32_bf16 v[124:127], a[124:127], v[204:207], v[124:127]\n\t"
    "ds_read_b64_tr_b16 a[108:109], v234 offset:2560\n\t"
    "ds_read_b64_tr_b16 a[110:111], v234 offset:35328\n\t"
    "L_entry_%=:\n\t"
    "v_mfma_f32_16x16x32_bf16 v[144:147], a[64:67], a[32:35], v[176:179]\n\t"
    "v_exp_f32 v209, v141\n\t"
    "v_mfma_f32_16x16x32_bf16 v[144:147], a[68:71], a[36:39], v[144:147]\n\t"
    "ds_read_b64_tr_b16 a[112:113], v233 offset:3072\n\t"
    "v_cvt_pk_bf16_f32 v198, v208, v209\n\t"
    "v_mfma_f32_16x16x32_bf16 v[144:147], a[72:75], a[40:43], v[144:147]\n\t"
    "v_exp_f32 v210, v142\n\t"
    "v_mfma_f32_16x16x32_bf16 v[144:147], a[76:79], a[44:47], v[144:147]\n\t"
    "buffer_load_dwordx4 v236, s[40:43], s56 offen offset:3072 lds\n\t"
    "v_mfma_f32_16x16x32_bf16 v[212:215], a[128:131], v[192:195], v[212:215]\n\t"
    "v_exp_f32 v211, v143\n\t"
    "v_mfma_f32_16x16x32_bf16 v[148:151], a[80:83], a[32:35], v[180:183]\n\t"
    "ds_read_b64_tr_b16 a[114:115], v233 offset:35840\n\t"
    "v_cvt_pk_bf16_f32 v199, v210, v211\n\t"
    "v_mfma_f32_16x16x32_bf16 v[148:151], a[84:87], a[36:39], v[148:151]\n\t"
    "ds_read_b64_tr_b16 a[116:117], v234 offset:3072\n\t"
    "ds_read_b64_tr_b16 a[118:119], v234 offset:35840\n\t"
    "v_mfma_f32_16x16x32_bf16 v[148:151], a[88:91], a[40:43], v[148:151]\n\t"
    "ds_read_b64_tr_b16 a[120:121], v233 offset:3584\n\t"
    "ds_read_b64_tr_b16 a[122:123], v233 offset:36352\n\t"
    "v_mfma_f32_16x16x32_bf16 v[148:151], a[92:95], a[44:47], v[148:151]\n\t"
    "v_exp_f32 v208, v144\n\t"
    "v_mfma_f32_16x16x32_bf16 v[216:219], a[128:131], v[196:199], v[216:219]\n\t"
    "ds_read_b64_tr_b16 a[124:125], v234 offset:3584\n\t"
    "ds_read_b64_tr_b16 a[126:127], v234 offset:36352\n\t"
    "v_mfma_f32_16x16x32_bf16 v[152:155], a[64:67], a[48:51], v[184:187]\n\t"
    "v_exp_f32 v209, v145\n\t"
    "s_add_u32 m0, m0, 0x10000\n\t"
    "v_mfma_f32_16x16x32_bf16 v[152:155], a[68:71], a[52:55], v[152:155]\n\t"
    "ds_read_b128 a[64:67], v232 offset:0\n\t"
    "v_cvt_pk_bf16_f32 v200, v208, v209\n\t"
    "v_mfma_f32_16x16x32_bf16 v[152:155], a[72:75], a[56:59], v[152:155]\n\t"
    "v_exp_f32 v210, v146\n\t"
    "v_mfma_f32_16x16x32_bf16 v[152:155], a[76:79], a[60:63], v[152:155]\n\t"
    "buffer_load_dwordx4 v235, s[44:47], s57 offen offset:0 lds\n\t"
    "v_mfma_f32_16x16x32_bf16 v[156:159], a[80:83], a[48:51], v[188:191]\n\t"
    "v_exp_f32 v211, v147\n\t"
    "v_add_f32 v241, v212, v216\n\t"
    "v_mfma_f32_16x16x32_bf16 v[156:159], a[84:87], a[52:55], v[156:159]\n\t"
    "ds_read_b128 a[68:71], v232 offset:512\n\t"
    "v_cvt_pk_bf16_f32 v201, v210, v211\n\t"
    "v_mfma_f32_16x16x32_bf16 v[156:159], a[88:91], a[56:59], v[156:159]\n\t"
    "v_exp_f32 v208, v148\n\t"
    "v_cmp_nge_f32 vcc, s64, v241\n\t"
    "v_mfma_f32_16x16x32_bf16 v[156:159], a[92:95], a[60:63], v[156:159]\n\t"
    "s_cbranch_vccnz L_stub0_%=\n\t"
    "L_ret0_%=:\n\t"
    "s_waitcnt lgkmcnt(2)\n\t"
    "v_mfma_f32_16x16x32_bf16 v[0:3], a[96:99], v[192:195], v[0:3]\n\t"
    "v_exp_f32 v209, v149\n\t"
    "v_mfma_f32_16x16x32_bf16 v[32:35], a[96:99], v[196:199], v[32:35]\n\t"
    "ds_read_b128 a[72:75], v232 offset:1024\n\t"
    "v_cvt_pk_bf16_f32 v202, v208, v209\n\t"
    "v_mfma_f32_16x16x32_bf16 v[4:7], a[100:103], v[192:195], v[4:7]\n\t"
    "v_exp_f32 v210, v150\n\t"
    "v_mfma_f32_16x16x32_bf16 v[36:39], a[100:103], v[196:199], v[36:39]\n\t"
    "buffer_load_dwordx4 v236, s[44:47], s57 offen offset:1024 lds\n\t"
    "v_mfma_f32_16x16x32_bf16 v[8:11], a[104:107], v[192:195], v[8:11]\n\t"
    "v_exp_f32 v211, v151\n\t"
    "v_mfma_f32_16x16x32_bf16 v[40:43], a[104:107], v[196:199], v[40:43]\n\t"
    "ds_read_b128 a[76:79], v232 offset:1536\n\t"
    "v_cvt_pk_bf16_f32 v203, v210, v211\n\t"
    "v_mfma_f32_16x16x32_bf16 v[12:15], a[108:111], v[192:195], v[12:15]\n\t"
    "v_exp_f32 v208, v152\n\t"
    "v_mfma_f32_16x16x32_bf16 v[44:47], a[108:111], v[196:199], v[44:47]\n\t"
    "ds_read_b128 a[80:83], v232 offset:32768\n\t"
    "ds_read_b128 a[84:87], v232 offset:33280\n\t"
    "v_mfma_f32_16x16x32_bf16 v[16:19], a[112:115], v[192:195], v[16:19]\n\t"
    "v_exp_f32 v209, v153\n\t"
    "v_mfma_f32_16x16x32_bf16 v[48:51], a[112:115], v[196:199], v[48:51]\n\t"
    "ds_read_b128 a[88:91], v232 offset:33792\n\t"
    "v_cvt_pk_bf16_f32 v204, v208, v209\n\t"
    "v_mfma_f32_16x16x32_bf16 v[20:23], a[116:119], v[192:195], v[20:23]\n\t"
    "v_exp_f32 v210, v154\n\t"
    "v_mfma_f32_16x16x32_bf16 v[52:55], a[116:119], v[196:199], v[52:55]\n\t"
    "buffer_load_dwordx4 v235, s[48:51], s57 offen offset:2048 lds\n\t"
    "v_mfma_f32_16x16x32_bf16 v[24:27], a[120:123], v[192:195], v[24:27]\n\t"
    "v_exp_f32 v211, v155\n\t"
    "v_mfma_f32_16x16x32_bf16 v[56:59], a[120:123], v[196:199], v[56:59]\n\t"
    "ds_read_b128 a[92:95], v232 offset:34304\n\t"
    "v_cvt_pk_bf16_f32 v205, v210, v211\n\t"
    "v_mfma_f32_16x16x32_bf16 v[28:31], a[124:127], v[192:195], v[28:31]\n\t"
    "v_exp_f32 v208, v156\n\t"
    "v_mfma_f32_16x16x32_bf16 v[60:63], a[124:127], v[196:199], v[60:63]\n\t"
    "s_and_b32 s59, s56, 0x6000\n\t"
    "s_add_u32 s56, s56, 0x2000\n\t"
    "s_waitcnt lgkmcnt(4)\n\t"
    "v_mfma_f32_16x16x32_bf16 v[128:131], a[64:67], a[0:3], v[160:163]\n\t"
    "v_exp_f32 v209, v157\n\t"
    "v_mfma_f32_16x16x32_bf16 v[128:131], a[68:71], a[4:7], v[128:131]\n\t"
    "v_cvt_pk_bf16_f32 v206, v208, v209\n\t"
    "v_bfi_b32 v232, v237, s56, v232\n\t"
    "v_mfma_f32_16x16x32_bf16 v[128:131], a[72:75], a[8:11], v[128:131]\n\t"
    "v_exp_f32 v210, v158\n\t"
    "v_mfma_f32_16x16x32_bf16 v[128:131], a[76:79], a[12:15], v[128:131]\n\t"
    "buffer_load_dwordx4 v236, s[48:51], s57 offen offset:3072 lds\n\t"
    "v_mfma_f32_16x16x32_bf16 v[220:223], a[128:131], v[200:203], v[220:223]\n\t"
    "v_exp_f32 v211, v159\n\t"
    "s_waitcnt vmcnt(16) lgkmcnt(0)\n\t"
    "v_mfma_f32_16x16x32_bf16 v[132:135], a[80:83], a[0:3], v[164:167]\n\t"
    "v_cvt_pk_bf16_f32 v207, v210, v211\n\t"
    "v_bfi_b32 v233, v237, s56, v233\n\t"
    "v_mfma_f32_16x16x32_bf16 v[132:135], a[84:87], a[4:7], v[132:135]\n\t"
    "s_barrier\n\t"
    "v_mfma_f32_16x16x32_bf16 v[132:135], a[88:91], a[8:11], v[132:135]\n\t"
    "s_sub_u32 s57, s57, 0x2000\n\t"
    "s_or_b32 m0, s59, s30\n\t"
    "v_mfma_f32_16x16x32_bf16 v[132:135], a[92:95], a[12:15], v[132:135]\n\t"
    "v_exp_f32 v208, v128\n\t"
    "v_mfma_f32_16x16x32_bf16 v[224:227], a[128:131], v[204:207], v[224:227]\n\t"
    "buffer_load_dwordx4 v235, s[36:39], s56 offen offset:0 lds\n\t"
    "v_mfma_f32_16x16x32_bf16 v[136:139], a[64:67], a[16:19], v[168:171]\n\t"
    "v_exp_f32 v209, v129\n\t"
    "v_mfma_f32_16x16x32_bf16 v[136:139], a[68:71], a[20:23], v[136:139]\n\t"
    "v_cvt_pk_bf16_f32 v192, v208, v209\n\t"
    "v_bfi_b32 v234, v237, s56, v234\n\t"
    "v_mfma_f32_16x16x32_bf16 v[136:139], a[72:75], a[24:27], v[136:139]\n\t"
    "v_exp_f32 v210, v130\n\t"
    "v_mfma_f32_16x16x32_bf16 v[136:139], a[76:79], a[28:31], v[136:139]\n\t"
    "s_cmp_eq_u32 s56, s58\n\t"
    "s_cselect_b32 s31, -1.0, s64\n\t"
    "v_mfma_f32_16x16x32_bf16 v[140:143], a[80:83], a[16:19], v[172:175]\n\t"
    "v_exp_f32 v211, v131\n\t"
    "v_mfma_f32_16x16x32_bf16 v[140:143], a[84:87], a[20:23], v[140:143]\n\t"
    "v_cvt_pk_bf16_f32 v193, v210, v211\n\t"
    "v_add_f32 v241, v220, v224\n\t"
    "v_mfma_f32_16x16x32_bf16 v[140:143], a[88:91], a[24:27], v[140:143]\n\t"
    "v_exp_f32 v208, v132\n\t"
    "v_cmp_nge_f32 vcc, s31, v241\n\t"
    "v_mfma_f32_16x16x32_bf16 v[140:143], a[92:95], a[28:31], v[140:143]\n\t"
    "s_cbranch_vccz L_loop_%=\n\t"
    "L_slow_%=:\n\t"
    "v_cmp_nge_f32 vcc, s64, v241\n\t"
    "s_cmp_eq_u32 s56, s58\n\t"
    "s_cbranch_vccnz L_stub1_%=\n\t"
    "L_ret1_%=:\n\t"
    "s_cbranch_scc0 L_loop_%=\n\t"
    "s_setpc_b64 s[62:63]\n\t"
    "L_hdiag_%=:\n\t"
    "s_mov_b64 s[66:67], s[70:71]\n\t"
    "L_hdiag2_%=:\n\t"
    "v_cvt_f32_i32 v242, v238\n\t"
    "v_add_f32 v242, 0.5, v242\n\t"
    "v_ldexp_f32 v242, v242, 64\n\t"
    "v_mov_b32 v246, 0xdf000000\n\t"
    "v_add_f32 v243, 0xdf800000, v242\n\t"
    "v_add_f32 v244, 0xe0000000, v242\n\t"
    "v_add_f32 v245, 0xe0400000, v242\n\t"
    "s_add_u32 s58, s58, 0x2000\n\t"
    "s_add_u32 s62, s70, s72\n\t"
    "s_addc_u32 s63, s71, 0\n\t"
    "v_min_f32 v176, v230, v242\n\t"
    "v_mov_b32 v180, v246\n\t"
    "v_min_f32 v188, v231, v242\n\t"
    "v_min_f32 v177, v230, v243\n\t"
    "v_mov_b32 v181, v246\n\t"
    "v_min_f32 v189, v231, v243\n\t"
    "v_min_f32 v178, v230, v244\n\t"
    "v_mov_b32 v182, v246\n\t"
    "v_min_f32 v190, v231, v244\n\t"
    "v_min_f32 v179, v230, v245\n\t"
    "v_mov_b32 v183, v246\n\t"
    "v_min_f32 v191, v231, v245\n\t"
    "v_min_f32 v160, v228, v242\n\t"
    "v_mov_b32 v164, v246\n\t"
    "v_min_f32 v172, v229, v242\n\t"
    "v_min_f32 v161, v228, v243\n\t"
    "v_mov_b32 v165, v246\n\t"
    "v_min_f32 v173, v229, v243\n\t"
    "v_min_f32 v162, v228, v244\n\t"
    "v_mov_b32 v166, v246\n\t"
    "v_min_f32 v174, v229, v244\n\t"
    "v_min_f32 v163, v228, v245\n\t"
    "v_mov_b32 v167, v246\n\t"
    "v_min_f32 v175, v229, v245\n\t"
    "s_setpc_b64 s[66:67]\n\t"
    "L_hfull_%=:\n\t"
    "s_mov_b32 s58, 311296\n\t"
    "s_add_u32 s62, s70, L_final_%=-L_loop_%=\n\t"
    "s_addc_u32 s63, s71, 0\n\t"
    "v_mov_b32 v176, v230\n\t"
    "v_mov_b32 v177, v230\n\t"
    "v_mov_b32 v178, v230\n\t"
    "v_mov_b32 v179, v230\n\t"
    "v_mov_b32 v180, v230\n\t"
    "v_mov_b32 v181, v230\n\t"
    "v_mov_b32 v182, v230\n\t"
    "v_mov_b32 v183, v230\n\t"
    "v_mov_b32 v188, v231\n\t"
    "v_mov_b32 v189, v231\n\t"
    "v_mov_b32 v190, v231\n\t"
    "v_mov_b32 v191, v231\n\t"
    "v_mov_b32 v160, v228\n\t"
    "v_mov_b32 v161, v228\n\t"
    "v_mov_b32 v162, v228\n\t"
    "v_mov_b32 v163, v228\n\t"
    "v_mov_b32 v164, v228\n\t"
    "v_mov_b32 v165, v228\n\t"
    "v_mov_b32 v166, v228\n\t"
    "v_mov_b32 v167, v228\n\t"
    "v_mov_b32 v172, v229\n\t"
    "v_mov_b32 v173, v229\n\t"
    "v_mov_b32 v174, v229\n\t"
    "v_mov_b32 v175, v229\n\t"
    "s_branch L_loop_%=\n\t"
    "L_hymask_%=:\n\t"
    "s_mov_b32 s58, 311296\n\t"
    "s_add_u32 s62, s70, L_final_%=-L_loop_%=\n\t"
    "s_addc_u32 s63, s71, 0\n\t"
    "v_mov_b32 v176, 0xdf000000\n\t"
    "v_mov_b32 v177, 0xdf000000\n\t"
    "v_mov_b32 v178, 0xdf000000\n\t"
    "v_mov_b32 v179, 0xdf000000\n\t"
    "v_mov_b32 v180, 0xdf000000\n\t"
    "v_mov_b32 v181, 0xdf000000\n\t"
    "v_mov_b32 v182, 0xdf000000\n\t"
    "v_mov_b32 v183, 0xdf000000\n\t"
    "v_mov_b32 v184, 0xdf000000\n\t"
    "v_mov_b32 v185, 0xdf000000\n\t"
    "v_mov_b32 v186, 0xdf000000\n\t"
    "v_mov_b32 v187, 0xdf000000\n\t"
    "v_mov_b32 v188, 0xdf000000\n\t"
    "v_mov_b32 v189, 0xdf000000\n\t"
    "v_mov_b32 v190, 0xdf000000\n\t"
    "v_mov_b32 v191, 0xdf000000\n\t"
    "s_branch L_loop_%=\n\t"
    "L_switch_%=:\n\t"
    "v_mfma_f32_16x16x32_bf16 v[64:67], a[96:99], v[200:203], v[64:67]\n\t"
    "v_exp_f32 v209, v133\n\t"
    "v_mfma_f32_16x16x32_bf16 v[96:99], a[96:99], v[204:207], v[96:99]\n\t"
    "ds_read_b64_tr_b16 a[96:97], v233 offset:2048\n\t"
    "v_cvt_pk_bf16_f32 v194, v208, v209\n\t"
    "v_mfma_f32_16x16x32_bf16 v[68:71], a[100:103], v[200:203], v[68:71]\n\t"
    "v_exp_f32 v210, v134\n\t"
    "v_mfma_f32_16x16x32_bf16 v[100:103], a[100:103], v[204:207], v[100:103]\n\t"
    "buffer_load_dwordx4 v236, s[36:39], s56 offen offset:1024 lds\n\t"
    "v_mfma_f32_16x16x32_bf16 v[72:75], a[104:107], v[200:203], v[72:75]\n\t"
    "v_exp_f32 v211, v135\n\t"
    "v_mfma_f32_16x16x32_bf16 v[104:107], a[104:107], v[204:207], v[104:107]\n\t"
    "ds_read_b64_tr_b16 a[98:99], v233 offset:34816\n\t"
    "v_cvt_pk_bf16_f32 v195, v210, v211\n\t"
    "v_mfma_f32_16x16x32_bf16 v[76:79], a[108:111], v[200:203], v[76:79]\n\t"
    "v_exp_f32 v208, v136\n\t"
    "v_mfma_f32_16x16x32_bf16 v[108:111], a[108:111], v[204:207], v[108:111]\n\t"
    "ds_read_b64_tr_b16 a[100:101], v234 offset:2048\n\t"
    "ds_read_b64_tr_b16 a[102:103], v234 offset:34816\n\t"
    "v_mfma_f32_16x16x32_bf16 v[80:83], a[112:115], v[200:203], v[80:83]\n\t"
    "v_exp_f32 v209, v137\n\t"
    "v_mfma_f32_16x16x32_bf16 v[112:115], a[112:115], v[204:207], v[112:115]\n\t"
    "ds_read_b64_tr_b16 a[104:105], v233 offset:2560\n\t"
    "v_cvt_pk_bf16_f32 v196, v208, v209\n\t"
    "v_mfma_f32_16x16x32_bf16 v[84:87], a[116:119], v[200:203], v[84:87]\n\t"
    "v_exp_f32 v210, v138\n\t"
    "v_mfma_f32_16x16x32_bf16 v[116:119], a[116:119], v[204:207], v[116:119]\n\t"
    "buffer_load_dwordx4 v235, s[40:43], s56 offen offset:2048 lds\n\t"
    "v_mfma_f32_16x16x32_bf16 v[88:91], a[120:123], v[200:203], v[88:91]\n\t"
    "v_exp_f32 v211, v139\n\t"
    "v_mfma_f32_16x16x32_bf16 v[120:123], a[120:123], v[204:207], v[120:123]\n\t"
    "ds_read_b64_tr_b16 a[106:107], v233 offset:35328\n\t"
    "v_cvt_pk_bf16_f32 v197, v210, v211\n\t"
    "v_mfma_f32_16x16x32_bf16 v[92:95], a[124:127], v[200:203], v[92:95]\n\t"
    "v_exp_f32 v208, v140\n\t"
    "v_mfma_f32_16x16x32_bf16 v[124:127], a[124:127], v[204:207], v[124:127]\n\t"
    "ds_read_b64_tr_b16 a[108:109], v234 offset:2560\n\t"
    "ds_read_b64_tr_b16 a[110:111], v234 offset:35328\n\t"
    "v_exp_f32 v209, v141\n\t"
    "ds_read_b64_tr_b16 a[112:113], v233 offset:3072\n\t"
    "v_cvt_pk_bf16_f32 v198, v208, v209\n\t"
    "v_exp_f32 v210, v142\n\t"
    "v_mfma_f32_16x16x32_bf16 v[212:215], a[128:131], v[192:195], v[212:215]\n\t"
    "v_exp_f32 v211, v143\n\t"
    "ds_read_b64_tr_b16 a[114:115], v233 offset:35840\n\t"
    "v_cvt_pk_bf16_f32 v199, v210, v211\n\t"
    "buffer_load_dwordx4 v236, s[40:43], s56 offen offset:3072 lds\n\t"
    "s_add_u32 m0, m0, 0x10000\n\t"
    "s_and_b32 s59, s56, 0x6000\n\t"
    "buffer_load_dwordx4 v235, s[44:47], s57 offen offset:0 lds\n\t"
    "buffer_load_dwordx4 v236, s[44:47], s57 offen offset:1024 lds\n\t"
    "buffer_load_dwordx4 v235, s[48:51], s57 offen offset:2048 lds\n\t"
    "buffer_load_dwordx4 v236, s[48:51], s57 offen offset:3072 lds\n\t"
    "s_add_u32 s56, s56, 0x2000\n\t"
    "s_sub_u32 s57, s57, 0x2000\n\t"
    "s_or_b32 m0, s59, s30\n\t"
    "ds_read_b64_tr_b16 a[116:117], v234 offset:3072\n\t"
    "ds_read_b64_tr_b16 a[118:119], v234 offset:35840\n\t"
    "ds_read_b64_tr_b16 a[120:121], v233 offset:3584\n\t"
    "ds_read_b64_tr_b16 a[122:123], v233 offset:36352\n\t"
    "v_mfma_f32_16x16x32_bf16 v[216:219], a[128:131], v[196:199], v[216:219]\n\t"
    "ds_read_b64_tr_b16 a[124:125], v234 offset:3584\n\t"
    "ds_read_b64_tr_b16 a[126:127], v234 offset:36352\n\t"
    "s_lshl_b32 s76, s68, 8\n\t"
    "s_add_u32 s77, s76, 4096\n\t"
    "s_lshl_b32 s78, s69, 8\n\t"
    "s_add_u32 s79, s78, 4096\n\t"
    "s_lshl_b32 s60, s74, 14\n\t"
    "s_sub_u32 s83, 147456, s60\n\t"
    "v_accvgpr_read_b32 v254, a132\n\t"
    "v_add_u32 v254, s83, v254\n\t"
    "v_rcp_f32 v144, v220\n\t"
    "v_rcp_f32 v146, v224\n\t"
    "v_add_f32 v241, v212, v216\n\t"
    "v_cmp_nge_f32 vcc, s64, v241\n\t"
    "s_cbranch_vccnz L_stub2_%=\n\t"
    "L_ret2_%=:\n\t"
    "v_rcp_f32 v148, v212\n\t"
    "v_rcp_f32 v150, v216\n\t"
    "s_waitcnt vmcnt(16) lgkmcnt(0)\n\t"
    "s_barrier\n\t"
    "v_mfma_f32_16x16x32_bf16 v[0:3], a[96:99], v[192:195], v[0:3]\n\t"
    "ds_read_b128 a[0:3], v254 offset:0\n\t"
    "v_and_b32 v190, 15, v239\n\t"
    "v_mfma_f32_16x16x32_bf16 v[32:35], a[96:99], v[196:199], v[32:35]\n\t"
    "ds_read_b128 a[4:7], v254 offset:512\n\t"
    "v_lshrrev_b32 v191, 5, v239\n\t"
    "v_xor_b32 v191, v191, v190\n\t"
    "v_mfma_f32_16x16x32_bf16 v[4:7], a[100:103], v[192:195], v[4:7]\n\t"
    "ds_read_b128 a[8:11], v254 offset:1024\n\t"
    "v_lshlrev_b32 v188, 8, v190\n\t"
    "v_mfma_f32_16x16x32_bf16 v[36:39], a[100:103], v[196:199], v[36:39]\n\t"
    "ds_read_b128 a[12:15], v254 offset:1536\n\t"
    "v_lshl_add_u32 v188, v191, 4, v188\n\t"
    "v_bfe_u32 v191, v239, 4, 1\n\t"
    "v_mfma_f32_16x16x32_bf16 v[8:11], a[104:107], v[192:195], v[8:11]\n\t"
    "ds_read_b128 a[16:19], v254 offset:4096\n\t"
    "v_lshl_add_u32 v188, v191, 3, v188\n\t"
    "v_mfma_f32_16x16x32_bf16 v[40:43], a[104:107], v[196:199], v[40:43]\n\t"
    "ds_read_b128 a[20:23], v254 offset:4608\n\t"
    "v_add_u32 v188, s83, v188\n\t"
    "v_lshrrev_b32 v191, 4, v239\n\t"
    "v_mfma_f32_16x16x32_bf16 v[12:15], a[108:111], v[192:195], v[12:15]\n\t"
    "ds_read_b128 a[24:27], v254 offset:5120\n\t"
    "v_xor_b32 v190, v190, v191\n\t"
    "v_mfma_f32_16x16x32_bf16 v[44:47], a[108:111], v[196:199], v[44:47]\n\t"
    "ds_read_b128 a[28:31], v254 offset:5632\n\t"
    "v_lshlrev_b32 v190, 4, v190\n\t"
    "v_lshl_add_u32 v184, v191, 8, v190\n\t"
    "v_mfma_f32_16x16x32_bf16 v[16:19], a[112:115], v[192:195], v[16:19]\n\t"
    "ds_read_b128 a[32:35], v254 offset:8192\n\t"
    "v_add_u32 v184, s83, v184\n\t"
    "v_mfma_f32_16x16x32_bf16 v[48:51], a[112:115], v[196:199], v[48:51]\n\t"
    "ds_read_b128 a[36:39], v254 offset:8704\n\t"
    "v_xor_b32 v185, 64, v184\n\t"
    "v_xor_b32 v186, 0x80, v184\n\t"
    "v_mfma_f32_16x16x32_bf16 v[20:23], a[116:119], v[192:195], v[20:23]\n\t"
    "ds_read_b128 a[40:43], v254 offset:9216\n\t"
    "v_xor_b32 v187, 0xc0, v184\n\t"
    "v_mfma_f32_16x16x32_bf16 v[52:55], a[116:119], v[196:199], v[52:55]\n\t"
    "ds_read_b128 a[44:47], v254 offset:9728\n\t"
    "v_xor_b32 v176, 0, v188\n\t"
    "v_xor_b32 v177, 32, v188\n\t"
    "v_mfma_f32_16x16x32_bf16 v[24:27], a[120:123], v[192:195], v[24:27]\n\t"
    "ds_read_b128 a[48:51], v254 offset:12288\n\t"
    "v_xor_b32 v178, 64, v188\n\t"
    "v_mfma_f32_16x16x32_bf16 v[56:59], a[120:123], v[196:199], v[56:59]\n\t"
    "ds_read_b128 a[52:55], v254 offset:12800\n\t"
    "v_xor_b32 v179, 96, v188\n\t"
    "v_xor_b32 v180, 128, v188\n\t"
    "v_mfma_f32_16x16x32_bf16 v[28:31], a[124:127], v[192:195], v[28:31]\n\t"
    "ds_read_b128 a[56:59], v254 offset:13312\n\t"
    "v_xor_b32 v181, 160, v188\n\t"
    "v_mfma_f32_16x16x32_bf16 v[60:63], a[124:127], v[196:199], v[60:63]\n\t"
    "ds_read_b128 a[60:63], v254 offset:13824\n\t"
    "v_xor_b32 v182, 192, v188\n\t"
    "v_xor_b32 v183, 224, v188\n\t"
    "buffer_load_dwordx4 v235, s[36:39], s56 offen offset:0 lds\n\t"
    "buffer_load_dwordx4 v236, s[36:39], s56 offen offset:1024 lds\n\t"
    "buffer_load_dwordx4 v235, s[40:43], s56 offen offset:2048 lds\n\t"
    "buffer_load_dwordx4 v236, s[40:43], s56 offen offset:3072 lds\n\t"
    "s_add_u32 m0, m0, 0x10000\n\t"
    "s_and_b32 s59, s56, 0x6000\n\t"
    "buffer_load_dwordx4 v235, s[44:47], s57 offen offset:0 lds\n\t"
    "buffer_load_dwordx4 v236, s[44:47], s57 offen offset:1024 lds\n\t"
    "buffer_load_dwordx4 v235, s[48:51], s57 offen offset:2048 lds\n\t"
    "buffer_load_dwordx4 v236, s[48:51], s57 offen offset:3072 lds\n\t"
    "s_add_u32 s56, s56, 0x2000\n\t"
    "s_sub_u32 s57, s57, 0x2000\n\t"
    "s_or_b32 m0, s59, s30\n\t"
    "s_waitcnt lgkmcnt(0)\n\t"
    "v_pk_mul_f32 v[64:65], v[64:65], v[144:145] op_sel_hi:[1,0]\n\t"
    "v_pk_mul_f32 v[66:67], v[66:67], v[144:145] op_sel_hi:[1,0]\n\t"
    "v_pk_mul_f32 v[68:69], v[68:69], v[144:145] op_sel_hi:[1,0]\n\t"
    "v_pk_mul_f32 v[70:71], v[70:71], v[144:145] op_sel_hi:[1,0]\n\t"
    "v_cvt_pk_bf16_f32 v64, v64, v65\n\t"
    "v_cvt_pk_bf16_f32 v65, v66, v67\n\t"
    "v_cvt_pk_bf16_f32 v66, v68, v69\n\t"
    "v_cvt_pk_bf16_f32 v67, v70, v71\n\t"
    "ds_write_b64 v176, v[64:65] offset:8192\n\t"
    "ds_write_b64 v177, v[66:67] offset:8192\n\t"
    "v_pk_mul_f32 v[72:73], v[72:73], v[144:145] op_sel_hi:[1,0]\n\t"
    "v_pk_mul_f32 v[74:75], v[74:75], v[144:145] op_sel_hi:[1,0]\n\t"
    "v_pk_mul_f32 v[76:77], v[76:77], v[144:145] op_sel_hi:[1,0]\n\t"
    "v_pk_mul_f32 v[78:79], v[78:79], v[144:145] op_sel_hi:[1,0]\n\t"
    "v_cvt_pk_bf16_f32 v72, v72, v73\n\t"
    "v_cvt_pk_bf16_f32 v73, v74, v75\n\t"
    "v_cvt_pk_bf16_f32 v74, v76, v77\n\t"
    "v_cvt_pk_bf16_f32 v75, v78, v79\n\t"
    "ds_write_b64 v178, v[72:73] offset:8192\n\t"
    "ds_write_b64 v179, v[74:75] offset:8192\n\t"
    "v_pk_mul_f32 v[80:81], v[80:81], v[144:145] op_sel_hi:[1,0]\n\t"
    "v_pk_mul_f32 v[82:83], v[82:83], v[144:145] op_sel_hi:[1,0]\n\t"
    "v_pk_mul_f32 v[84:85], v[84:85], v[144:145] op_sel_hi:[1,0]\n\t"
    "v_pk_mul_f32 v[86:87], v[86:87], v[144:145] op_sel_hi:[1,0]\n\t"
    "v_cvt_pk_bf16_f32 v80, v80, v81\n\t"
    "v_cvt_pk_bf16_f32 v81, v82, v83\n\t"
    "v_cvt_pk_bf16_f32 v82, v84, v85\n\t"
    "v_cvt_pk_bf16_f32 v83, v86, v87\n\t"
    "ds_write_b64 v180, v[80:81] offset:8192\n\t"
    "ds_write_b64 v181, v[82:83] offset:8192\n\t"
    "v_pk_mul_f32 v[88:89], v[88:89], v[144:145] op_sel_hi:[1,0]\n\t"
    "v_pk_mul_f32 v[90:91], v[90:91], v[144:145] op_sel_hi:[1,0]\n\t"
    "v_pk_mul_f32 v[92:93], v[92:93], v[144:145] op_sel_hi:[1,0]\n\t"
    "v_pk_mul_f32 v[94:95], v[94:95], v[144:145] op_sel_hi:[1,0]\n\t"
    "v_cvt_pk_bf16_f32 v88, v88, v89\n\t"
    "v_cvt_pk_bf16_f32 v89, v90, v91\n\t"
    "v_cvt_pk_bf16_f32 v90, v92, v93\n\t"
    "v_cvt_pk_bf16_f32 v91, v94, v95\n\t"
    "ds_write_b64 v182, v[88:89] offset:8192\n\t"
    "ds_write_b64 v183, v[90:91] offset:8192\n\t"
    "v_pk_mul_f32 v[96:97], v[96:97], v[146:147] op_sel_hi:[1,0]\n\t"
    "v_pk_mul_f32 v[98:99], v[98:99], v[146:147] op_sel_hi:[1,0]\n\t"
    "v_pk_mul_f32 v[100:101], v[100:101], v[146:147] op_sel_hi:[1,0]\n\t"
    "v_pk_mul_f32 v[102:103], v[102:103], v[146:147] op_sel_hi:[1,0]\n\t"
    "v_cvt_pk_bf16_f32 v96, v96, v97\n\t"
    "v_cvt_pk_bf16_f32 v97, v98, v99\n\t"
    "v_cvt_pk_bf16_f32 v98, v100, v101\n\t"
    "v_cvt_pk_bf16_f32 v99, v102, v103\n\t"
    "ds_write_b64 v176, v[96:97] offset:12288\n\t"
    "ds_write_b64 v177, v[98:99] offset:12288\n\t"
    "v_pk_mul_f32 v[104:105], v[104:105], v[146:147] op_sel_hi:[1,0]\n\t"
    "v_pk_mul_f32 v[106:107], v[106:107], v[146:147] op_sel_hi:[1,0]\n\t"
    "v_pk_mul_f32 v[108:109], v[108:109], v[146:147] op_sel_hi:[1,0]\n\t"
    "v_pk_mul_f32 v[110:111], v[110:111], v[146:147] op_sel_hi:[1,0]\n\t"
    "v_cvt_pk_bf16_f32 v104, v104, v105\n\t"
    "v_cvt_pk_bf16_f32 v105, v106, v107\n\t"
    "v_cvt_pk_bf16_f32 v106, v108, v109\n\t"
    "v_cvt_pk_bf16_f32 v107, v110, v111\n\t"
    "ds_write_b64 v178, v[104:105] offset:12288\n\t"
    "ds_write_b64 v179, v[106:107] offset:12288\n\t"
    "s_waitcnt lgkmcnt(4)\n\t"
    "ds_read_b128 v[64:67], v184 offset:8192\n\t"
    "ds_read_b128 v[68:71], v185 offset:9216\n\t"
    "ds_read_b128 v[72:75], v186 offset:10240\n\t"
    "ds_read_b128 v[76:79], v187 offset:11264\n\t"
    "v_pk_mul_f32 v[112:113], v[112:113], v[146:147] op_sel_hi:[1,0]\n\t"
    "v_pk_mul_f32 v[114:115], v[114:115], v[146:147] op_sel_hi:[1,0]\n\t"
    "v_pk_mul_f32 v[116:117], v[116:117], v[146:147] op_sel_hi:[1,0]\n\t"
    "v_pk_mul_f32 v[118:119], v[118:119], v[146:147] op_sel_hi:[1,0]\n\t"
    "v_cvt_pk_bf16_f32 v112, v112, v113\n\t"
    "v_cvt_pk_bf16_f32 v113, v114, v115\n\t"
    "v_cvt_pk_bf16_f32 v114, v116, v117\n\t"
    "v_cvt_pk_bf16_f32 v115, v118, v119\n\t"
    "ds_write_b64 v180, v[112:113] offset:12288\n\t"
    "ds_write_b64 v181, v[114:115] offset:12288\n\t"
    "v_pk_mul_f32 v[120:121], v[120:121], v[146:147] op_sel_hi:[1,0]\n\t"
    "v_pk_mul_f32 v[122:123], v[122:123], v[146:147] op_sel_hi:[1,0]\n\t"
    "v_pk_mul_f32 v[124:125], v[124:125], v[146:147] op_sel_hi:[1,0]\n\t"
    "v_pk_mul_f32 v[126:127], v[126:127], v[146:147] op_sel_hi:[1,0]\n\t"
    "v_cvt_pk_bf16_f32 v120, v120, v121\n\t"
    "v_cvt_pk_bf16_f32 v121, v122, v123\n\t"
    "v_cvt_pk_bf16_f32 v122, v124, v125\n\t"
    "v_cvt_pk_bf16_f32 v123, v126, v127\n\t"
    "ds_write_b64 v182, v[120:121] offset:12288\n\t"
    "ds_write_b64 v183, v[122:123] offset:12288\n\t"
    "s_waitcnt vmcnt(16)\n\t"
    "s_barrier\n\t"
    "s_waitcnt lgkmcnt(4)\n\t"
    "buffer_store_dwordx4 v[64:67], v240, s[52:55], s78 offen offset:0 nt\n\t"
    "buffer_store_dwordx4 v[68:71], v240, s[52:55], s78 offen offset:1024 nt\n\t"
    "buffer_store_dwordx4 v[72:75], v240, s[52:55], s78 offen offset:2048 nt\n\t"
    "buffer_store_dwordx4 v[76:79], v240, s[52:55], s78 offen offset:3072 nt\n\t"
    "v_pk_mul_f32 v[0:1], v[0:1], v[148:149] op_sel_hi:[1,0]\n\t"
    "v_pk_mul_f32 v[2:3], v[2:3], v[148:149] op_sel_hi:[1,0]\n\t"
    "v_pk_mul_f32 v[4:5], v[4:5], v[148:149] op_sel_hi:[1,0]\n\t"
    "v_pk_mul_f32 v[6:7], v[6:7], v[148:149] op_sel_hi:[1,0]\n\t"
    "v_cvt_pk_bf16_f32 v0, v0, v1\n\t"
    "v_cvt_pk_bf16_f32 v1, v2, v3\n\t"
    "v_cvt_pk_bf16_f32 v2, v4, v5\n\t"
    "v_cvt_pk_bf16_f32 v3, v6, v7\n\t"
    "ds_write_b64 v176, v[0:1] offset:0\n\t"
    "ds_write_b64 v177, v[2:3] offset:0\n\t"
    "v_pk_mul_f32 v[8:9], v[8:9], v[148:149] op_sel_hi:[1,0]\n\t"
    "v_pk_mul_f32 v[10:11], v[10:11], v[148:149] op_sel_hi:[1,0]\n\t"
    "v_pk_mul_f32 v[12:13], v[12:13], v[148:149] op_sel_hi:[1,0]\n\t"
    "v_pk_mul_f32 v[14:15], v[14:15], v[148:149] op_sel_hi:[1,0]\n\t"
    "v_cvt_pk_bf16_f32 v8, v8, v9\n\t"
    "v_cvt_pk_bf16_f32 v9, v10, v11\n\t"
    "v_cvt_pk_bf16_f32 v10, v12, v13\n\t"
    "v_cvt_pk_bf16_f32 v11, v14, v15\n\t"
    "ds_write_b64 v178, v[8:9] offset:0\n\t"
    "ds_write_b64 v179, v[10:11] offset:0\n\t"
    "s_waitcnt lgkmcnt(4)\n\t"
    "ds_read_b128 v[96:99], v184 offset:12288\n\t"
    "ds_read_b128 v[100:103], v185 offset:13312\n\t"
    "ds_read_b128 v[104:107], v186 offset:14336\n\t"
    "ds_read_b128 v[108:111], v187 offset:15360\n\t"
    "v_pk_mul_f32 v[16:17], v[16:17], v[148:149] op_sel_hi:[1,0]\n\t"
    "v_pk_mul_f32 v[18:19], v[18:19], v[148:149] op_sel_hi:[1,0]\n\t"
    "v_pk_mul_f32 v[20:21], v[20:21], v[148:149] op_sel_hi:[1,0]\n\t"
    "v_pk_mul_f32 v[22:23], v[22:23], v[148:149] op_sel_hi:[1,0]\n\t"
    "v_cvt_pk_bf16_f32 v16, v16, v17\n\t"
    "v_cvt_pk_bf16_f32 v17, v18, v19\n\t"
    "v_cvt_pk_bf16_f32 v18, v20, v21\n\t"
    "v_cvt_pk_bf16_f32 v19, v22, v23\n\t"
    "ds_write_b64 v180, v[16:17] offset:0\n\t"
    "ds_write_b64 v181, v[18:19] offset:0\n\t"
    "v_pk_mul_f32 v[24:25], v[24:25], v[148:149] op_sel_hi:[1,0]\n\t"
    "v_pk_mul_f32 v[26:27], v[26:27], v[148:149] op_sel_hi:[1,0]\n\t"
    "v_pk_mul_f32 v[28:29], v[28:29], v[148:149] op_sel_hi:[1,0]\n\t"
    "v_pk_mul_f32 v[30:31], v[30:31], v[148:149] op_sel_hi:[1,0]\n\t"
    "v_cvt_pk_bf16_f32 v24, v24, v25\n\t"
    "v_cvt_pk_bf16_f32 v25, v26, v27\n\t"
    "v_cvt_pk_bf16_f32 v26, v28, v29\n\t"
    "v_cvt_pk_bf16_f32 v27, v30, v31\n\t"
    "ds_write_b64 v182, v[24:25] offset:0\n\t"
    "ds_write_b64 v183, v[26:27] offset:0\n\t"
    "s_waitcnt lgkmcnt(4)\n\t"
    "buffer_store_dwordx4 v[96:99], v240, s[52:55], s79 offen offset:0 nt\n\t"
    "buffer_store_dwordx4 v[100:103], v240, s[52:55], s79 offen offset:1024 nt\n\t"
    "buffer_store_dwordx4 v[104:107], v240, s[52:55], s79 offen offset:2048 nt\n\t"
    "buffer_store_dwordx4 v[108:111], v240, s[52:55], s79 offen offset:3072 nt\n\t"
    "v_pk_mul_f32 v[32:33], v[32:33], v[150:151] op_sel_hi:[1,0]\n\t"
    "v_pk_mul_f32 v[34:35], v[34:35], v[150:151] op_sel_hi:[1,0]\n\t"
    "v_pk_mul_f32 v[36:37], v[36:37], v[150:151] op_sel_hi:[1,0]\n\t"
    "v_pk_mul_f32 v[38:39], v[38:39], v[150:151] op_sel_hi:[1,0]\n\t"
    "v_cvt_pk_bf16_f32 v32, v32, v33\n\t"
    "v_cvt_pk_bf16_f32 v33, v34, v35\n\t"
    "v_cvt_pk_bf16_f32 v34, v36, v37\n\t"
    "v_cvt_pk_bf16_f32 v35, v38, v39\n\t"
    "ds_write_b64 v176, v[32:33] offset:4096\n\t"
    "ds_write_b64 v177, v[34:35] offset:4096\n\t"
    "v_pk_mul_f32 v[40:41], v[40:41], v[150:151] op_sel_hi:[1,0]\n\t"
    "v_pk_mul_f32 v[42:43], v[42:43], v[150:151] op_sel_hi:[1,0]\n\t"
    "v_pk_mul_f32 v[44:45], v[44:45], v[150:151] op_sel_hi:[1,0]\n\t"
    "v_pk_mul_f32 v[46:47], v[46:47], v[150:151] op_sel_hi:[1,0]\n\t"
    "v_cvt_pk_bf16_f32 v40, v40, v41\n\t"
    "v_cvt_pk_bf16_f32 v41, v42, v43\n\t"
    "v_cvt_pk_bf16_f32 v42, v44, v45\n\t"
    "v_cvt_pk_bf16_f32 v43, v46, v47\n\t"
    "ds_write_b64 v178, v[40:41] offset:4096\n\t"
    "ds_write_b64 v179, v[42:43] offset:4096\n\t"
    "s_waitcnt lgkmcnt(4)\n\t"
    "ds_read_b128 v[0:3], v184 offset:0\n\t"
    "ds_read_b128 v[4:7], v185 offset:1024\n\t"
    "ds_read_b128 v[8:11], v186 offset:2048\n\t"
    "ds_read_b128 v[12:15], v187 offset:3072\n\t"
    "v_pk_mul_f32 v[48:49], v[48:49], v[150:151] op_sel_hi:[1,0]\n\t"
    "v_pk_mul_f32 v[50:51], v[50:51], v[150:151] op_sel_hi:[1,0]\n\t"
    "v_pk_mul_f32 v[52:53], v[52:53], v[150:151] op_sel_hi:[1,0]\n\t"
    "v_pk_mul_f32 v[54:55], v[54:55], v[150:151] op_sel_hi:[1,0]\n\t"
    "v_cvt_pk_bf16_f32 v48, v48, v49\n\t"
    "v_cvt_pk_bf16_f32 v49, v50, v51\n\t"
    "v_cvt_pk_bf16_f32 v50, v52, v53\n\t"
    "v_cvt_pk_bf16_f32 v51, v54, v55\n\t"
    "ds_write_b64 v180, v[48:49] offset:4096\n\t"
    "ds_write_b64 v181, v[50:51] offset:4096\n\t"
    "v_pk_mul_f32 v[56:57], v[56:57], v[150:151] op_sel_hi:[1,0]\n\t"
    "v_pk_mul_f32 v[58:59], v[58:59], v[150:151] op_sel_hi:[1,0]\n\t"
    "v_pk_mul_f32 v[60:61], v[60:61], v[150:151] op_sel_hi:[1,0]\n\t"
    "v_pk_mul_f32 v[62:63], v[62:63], v[150:151] op_sel_hi:[1,0]\n\t"
    "v_cvt_pk_bf16_f32 v56, v56, v57\n\t"
    "v_cvt_pk_bf16_f32 v57, v58, v59\n\t"
    "v_cvt_pk_bf16_f32 v58, v60, v61\n\t"
    "v_cvt_pk_bf16_f32 v59, v62, v63\n\t"
    "ds_write_b64 v182, v[56:57] offset:4096\n\t"
    "ds_write_b64 v183, v[58:59] offset:4096\n\t"
    "v_add_u32 v232, 65536, v232\n\t"
    "v_add_u32 v233, 65536, v233\n\t"
    "v_add_u32 v234, 65536, v234\n\t"
    "v_bfi_b32 v232, v237, s56, v232\n\t"
    "v_mov_b32 v228, 0\n\t"
    "v_mov_b32 v229, 0\n\t"
    "v_mov_b32 v230, 0\n\t"
    "v_mov_b32 v231, 0\n\t"
    "s_waitcnt lgkmcnt(4)\n\t"
    "buffer_store_dwordx4 v[0:3], v240, s[52:55], s76 offen offset:0 nt\n\t"
    "buffer_store_dwordx4 v[4:7], v240, s[52:55], s76 offen offset:1024 nt\n\t"
    "buffer_store_dwordx4 v[8:11], v240, s[52:55], s76 offen offset:2048 nt\n\t"
    "buffer_store_dwordx4 v[12:15], v240, s[52:55], s76 offen offset:3072 nt\n\t"
    "s_waitcnt lgkmcnt(0)\n\t"
    "ds_read_b128 v[32:35], v184 offset:4096\n\t"
    "ds_read_b128 v[36:39], v185 offset:5120\n\t"
    "ds_read_b128 v[40:43], v186 offset:6144\n\t"
    "ds_read_b128 v[44:47], v187 offset:7168\n\t"
    "ds_read_b128 a[64:67], v232 offset:0\n\t"
    "ds_read_b128 a[68:71], v232 offset:512\n\t"
    "ds_read_b128 a[72:75], v232 offset:1024\n\t"
    "ds_read_b128 a[76:79], v232 offset:1536\n\t"
    "ds_read_b128 a[80:83], v232 offset:32768\n\t"
    "ds_read_b128 a[84:87], v232 offset:33280\n\t"
    "ds_read_b128 a[88:91], v232 offset:33792\n\t"
    "ds_read_b128 a[92:95], v232 offset:34304\n\t"
    "buffer_load_dwordx4 v235, s[36:39], s56 offen offset:0 lds\n\t"
    "buffer_load_dwordx4 v236, s[36:39], s56 offen offset:1024 lds\n\t"
    "buffer_load_dwordx4 v235, s[40:43], s56 offen offset:2048 lds\n\t"
    "buffer_load_dwordx4 v236, s[40:43], s56 offen offset:3072 lds\n\t"
    "s_add_u32 m0, m0, 0x10000\n\t"
    "s_and_b32 s59, s56, 0x6000\n\t"
    "buffer_load_dwordx4 v235, s[44:47], s57 offen offset:0 lds\n\t"
    "buffer_load_dwordx4 v236, s[44:47], s57 offen offset:1024 lds\n\t"
    "buffer_load_dwordx4 v235, s[48:51], s57 offen offset:2048 lds\n\t"
    "buffer_load_dwordx4 v236, s[48:51], s57 offen offset:3072 lds\n\t"
    "s_add_u32 s56, s56, 0x2000\n\t"
    "s_sub_u32 s57, s57, 0x2000\n\t"
    "s_or_b32 m0, s59, s30\n\t"
    "v_mfma_f32_32x32x16_bf16 v[192:207], v[228:231], v[228:231], 0\n\t"
    "v_mov_b32 v208, 0\n\t"
    "v_mov_b32 v209, 0\n\t"
    "v_mov_b32 v210, 0\n\t"
    "v_mfma_f32_32x32x16_bf16 v[212:227], v[228:231], v[228:231], 0\n\t"
    "v_mov_b32 v211, 0\n\t"
    "v_mov_b32 v133, 0xdf000000\n\t"
    "v_mov_b32 v134, v133\n\t"
    "v_mfma_f32_32x32x16_bf16 v[64:79], v[228:231], v[228:231], 0\n\t"
    "v_mov_b32 v135, v133\n\t"
    "v_mov_b32 v136, v133\n\t"
    "v_mov_b32 v137, v133\n\t"
    "v_mfma_f32_32x32x16_bf16 v[80:95], v[228:231], v[228:231], 0\n\t"
    "v_mov_b32 v138, v133\n\t"
    "v_mov_b32 v139, v133\n\t"
    "v_mov_b32 v140, v133\n\t"
    "v_mfma_f32_32x32x16_bf16 v[96:111], v[228:231], v[228:231], 0\n\t"
    "v_mov_b32 v141, v133\n\t"
    "v_mov_b32 v142, v133\n\t"
    "v_mov_b32 v143, v133\n\t"
    "v_mfma_f32_32x32x16_bf16 v[112:127], v[228:231], v[228:231], 0\n\t"
    "v_cvt_f32_i32 v242, v238\n\t"
    "v_add_f32 v242, 0.5, v242\n\t"
    "v_ldexp_f32 v242, v242, 64\n\t"
    "v_mfma_f32_32x32x16_bf16 v[0:15], v[228:231], v[228:231], 0\n\t"
    "v_mov_b32 v246, 0xdf000000\n\t"
    "v_add_f32 v243, 0xdf800000, v242\n\t"
    "v_add_f32 v244, 0xe0000000, v242\n\t"
    "v_mfma_f32_32x32x16_bf16 v[16:31], v[228:231], v[228:231], 0\n\t"
    "v_add_f32 v245, 0xe0400000, v242\n\t"
    "v_bfi_b32 v232, v237, s56, v232\n\t"
    "v_bfi_b32 v233, v237, s56, v233\n\t"
    "v_bfi_b32 v234, v237, s56, v234\n\t"
    "s_waitcnt lgkmcnt(8)\n\t"
    "buffer_store_dwordx4 v[32:35], v240, s[52:55], s77 offen offset:0 nt\n\t"
    "buffer_store_dwordx4 v[36:39], v240, s[52:55], s77 offen offset:1024 nt\n\t"
    "buffer_store_dwordx4 v[40:43], v240, s[52:55], s77 offen offset:2048 nt\n\t"
    "buffer_store_dwordx4 v[44:47], v240, s[52:55], s77 offen offset:3072 nt\n\t"
    "s_waitcnt vmcnt(32) lgkmcnt(0)\n\t"
    "s_barrier\n\t"
    "buffer_load_dwordx4 v235, s[36:39], s56 offen offset:0 lds\n\t"
    "v_mfma_f32_32x32x16_bf16 v[32:47], v[228:231], v[228:231], 0\n\t"
    "v_mfma_f32_32x32x16_bf16 v[48:63], v[228:231], v[228:231], 0\n\t"
    "v_accvgpr_read_b32 v228, a136\n\t"
    "v_accvgpr_read_b32 v229, a137\n\t"
    "v_accvgpr_read_b32 v230, a138\n\t"
    "v_accvgpr_read_b32 v231, a139\n\t"
    "s_lshl_b32 s76, s75, 1\n\t"
    "s_add_u32 s76, s76, s74\n\t"
    "s_sub_u32 s76, 31, s76\n\t"
    "s_lshl_b32 s68, s76, 6\n\t"
    "s_add_u32 s69, s68, 32\n\t"
    "s_add_u32 s58, s56, 0x2000\n\t"
    "s_add_u32 s62, s70, L_hfull_%=-L_loop_%=\n\t"
    "s_addc_u32 s63, s71, 0\n\t"
    "v_min_f32 v176, v230, v242\n\t"
    "v_mov_b32 v180, v246\n\t"
    "v_min_f32 v188, v231, v242\n\t"
    "v_min_f32 v177, v230, v243\n\t"
    "v_mov_b32 v181, v246\n\t"
    "v_min_f32 v189, v231, v243\n\t"
    "v_min_f32 v178, v230, v244\n\t"
    "v_mov_b32 v182, v246\n\t"
    "v_min_f32 v190, v231, v244\n\t"
    "v_min_f32 v179, v230, v245\n\t"
    "v_mov_b32 v183, v246\n\t"
    "v_min_f32 v191, v231, v245\n\t"
    "v_min_f32 v160, v228, v242\n\t"
    "v_mov_b32 v164, v246\n\t"
    "v_min_f32 v172, v229, v242\n\t"
    "v_min_f32 v161, v228, v243\n\t"
    "v_mov_b32 v165, v246\n\t"
    "v_min_f32 v173, v229, v243\n\t"
    "v_min_f32 v162, v228, v244\n\t"
    "v_mov_b32 v166, v246\n\t"
    "v_min_f32 v174, v229, v244\n\t"
    "v_min_f32 v163, v228, v245\n\t"
    "v_mov_b32 v167, v246\n\t"
    "v_min_f32 v175, v229, v245\n\t"
    "v_mov_b32 v184, v231\n\t"
    "v_mov_b32 v185, v231\n\t"
    "v_mov_b32 v186, v231\n\t"
    "v_mov_b32 v187, v231\n\t"
    "v_mov_b32 v168, v229\n\t"
    "v_mov_b32 v169, v229\n\t"
    "v_mov_b32 v170, v229\n\t"
    "v_mov_b32 v171, v229\n\t"
    "s_cmp_eq_u32 s56, 311296\n\t"
    "s_cbranch_scc0 L_loop_%=\n\t"
    "L_final_%=:\n\t"
    "v_add_u32 v250, s30, v240\n\t"
    "v_xor_b32 v251, 0x9000, v250\n\t"
    "v_add_u32 v252, 65536, v250\n\t"
    "v_add_u32 v253, 65536, v251\n\t"
    "v_mov_b32 v242, v228\n\t"
    "v_mov_b32 v243, v229\n\t"
    "v_mov_b32 v244, v212\n\t"
    "v_mov_b32 v245, v216\n\t"
    "s_lshl_b32 s83, s74, 13\n\t"
    "s_add_u32 s83, s83, 131072\n\t"
    "s_waitcnt vmcnt(0)\n\t"
    "v_mfma_f32_16x16x32_bf16 v[64:67], a[96:99], v[200:203], v[64:67]\n\t"
    "ds_write_b128 v252, v[242:245] offset:16384\n\t"
    "v_mfma_f32_16x16x32_bf16 v[96:99], a[96:99], v[204:207], v[96:99]\n\t"
    "ds_write_b128 v250, v[0:3] offset:0\n\t"
    "v_mfma_f32_16x16x32_bf16 v[68:71], a[100:103], v[200:203], v[68:71]\n\t"
    "ds_write_b128 v250, v[4:7] offset:1024\n\t"
    "v_mfma_f32_16x16x32_bf16 v[100:103], a[100:103], v[204:207], v[100:103]\n\t"
    "ds_write_b128 v250, v[8:11] offset:2048\n\t"
    "v_mfma_f32_16x16x32_bf16 v[72:75], a[104:107], v[200:203], v[72:75]\n\t"
    "ds_write_b128 v250, v[12:15] offset:3072\n\t"
    "v_mfma_f32_16x16x32_bf16 v[104:107], a[104:107], v[204:207], v[104:107]\n\t"
    "ds_write_b128 v250, v[16:19] offset:16384\n\t"
    "v_mfma_f32_16x16x32_bf16 v[76:79], a[108:111], v[200:203], v[76:79]\n\t"
    "ds_write_b128 v250, v[20:23] offset:17408\n\t"
    "v_mfma_f32_16x16x32_bf16 v[108:111], a[108:111], v[204:207], v[108:111]\n\t"
    "ds_write_b128 v250, v[24:27] offset:18432\n\t"
    "v_mfma_f32_16x16x32_bf16 v[80:83], a[112:115], v[200:203], v[80:83]\n\t"
    "ds_write_b128 v250, v[28:31] offset:19456\n\t"
    "v_mfma_f32_16x16x32_bf16 v[112:115], a[112:115], v[204:207], v[112:115]\n\t"
    "ds_write_b128 v250, v[32:35] offset:24576\n\t"
    "v_mfma_f32_16x16x32_bf16 v[84:87], a[116:119], v[200:203], v[84:87]\n\t"
    "ds_write_b128 v250, v[36:39] offset:25600\n\t"
    "v_mfma_f32_16x16x32_bf16 v[116:119], a[116:119], v[204:207], v[116:119]\n\t"
    "ds_write_b128 v250, v[40:43] offset:26624\n\t"
    "v_mfma_f32_16x16x32_bf16 v[88:91], a[120:123], v[200:203], v[88:91]\n\t"
    "ds_write_b128 v250, v[44:47] offset:27648\n\t"
    "v_mfma_f32_16x16x32_bf16 v[120:123], a[120:123], v[204:207], v[120:123]\n\t"
    "ds_write_b128 v252, v[48:51] offset:0\n\t"
    "v_mfma_f32_16x16x32_bf16 v[92:95], a[124:127], v[200:203], v[92:95]\n\t"
    "ds_write_b128 v252, v[52:55] offset:1024\n\t"
    "v_mfma_f32_16x16x32_bf16 v[124:127], a[124:127], v[204:207], v[124:127]\n\t"
    "ds_write_b128 v252, v[56:59] offset:2048\n\t"
    "ds_write_b128 v252, v[60:63] offset:3072\n\t"
    "s_lshl_b32 s76, s69, 8\n\t"
    "s_add_u32 s77, s76, 4096\n\t"
    "v_and_b32 v190, 15, v239\n\t"
    "v_lshrrev_b32 v191, 5, v239\n\t"
    "v_xor_b32 v191, v191, v190\n\t"
    "v_lshlrev_b32 v188, 8, v190\n\t"
    "v_lshl_add_u32 v188, v191, 4, v188\n\t"
    "v_bfe_u32 v191, v239, 4, 1\n\t"
    "v_lshl_add_u32 v188, v191, 3, v188\n\t"
    "v_add_u32 v188, s83, v188\n\t"
    "v_lshrrev_b32 v191, 4, v239\n\t"
    "v_xor_b32 v190, v190, v191\n\t"
    "v_lshlrev_b32 v190, 4, v190\n\t"
    "v_lshl_add_u32 v184, v191, 8, v190\n\t"
    "v_add_u32 v184, s83, v184\n\t"
    "v_xor_b32 v185, 64, v184\n\t"
    "v_xor_b32 v186, 0x80, v184\n\t"
    "v_xor_b32 v187, 0xc0, v184\n\t"
    "v_xor_b32 v176, 0, v188\n\t"
    "v_xor_b32 v177, 32, v188\n\t"
    "v_xor_b32 v178, 64, v188\n\t"
    "v_xor_b32 v179, 96, v188\n\t"
    "v_xor_b32 v180, 128, v188\n\t"
    "v_xor_b32 v181, 160, v188\n\t"
    "v_xor_b32 v182, 192, v188\n\t"
    "v_xor_b32 v183, 224, v188\n\t"
    "s_waitcnt lgkmcnt(0)\n\t"
    "s_barrier\n\t"
    "ds_read_b128 v[242:245], v253 offset:16384\n\t"
    "ds_read_b128 v[0:3], v251 offset:0\n\t"
    "ds_read_b128 v[4:7], v251 offset:1024\n\t"
    "ds_read_b128 v[8:11], v251 offset:2048\n\t"
    "ds_read_b128 v[12:15], v251 offset:3072\n\t"
    "ds_read_b128 v[16:19], v251 offset:16384\n\t"
    "ds_read_b128 v[20:23], v251 offset:17408\n\t"
    "ds_read_b128 v[24:27], v251 offset:18432\n\t"
    "ds_read_b128 v[28:31], v251 offset:19456\n\t"
    "ds_read_b128 v[32:35], v251 offset:24576\n\t"
    "ds_read_b128 v[36:39], v251 offset:25600\n\t"
    "ds_read_b128 v[40:43], v251 offset:26624\n\t"
    "ds_read_b128 v[44:47], v251 offset:27648\n\t"
    "ds_read_b128 v[48:51], v253 offset:0\n\t"
    "ds_read_b128 v[52:55], v253 offset:1024\n\t"
    "s_waitcnt lgkmcnt(14)\n\t"
    "ds_read_b128 v[56:59], v253 offset:2048\n\t"
    "ds_read_b128 v[60:63], v253 offset:3072\n\t"
    "v_min_f32 v246, v230, v242\n\t"
    "v_min_f32 v247, v231, v243\n\t"
    "v_sub_f32 v144, v246, v230\n\t"
    "v_sub_f32 v146, v247, v231\n\t"
    "v_sub_f32 v148, v246, v242\n\t"
    "v_sub_f32 v150, v247, v243\n\t"
    "v_exp_f32 v144, v144\n\t"
    "v_exp_f32 v146, v146\n\t"
    "v_exp_f32 v148, v148\n\t"
    "v_exp_f32 v150, v150\n\t"
    "v_mul_f32 v246, v144, v220\n\t"
    "v_mul_f32 v247, v146, v224\n\t"
    "v_fmac_f32 v246, v148, v244\n\t"
    "v_fmac_f32 v247, v150, v245\n\t"
    "v_rcp_f32 v246, v246\n\t"
    "v_rcp_f32 v247, v247\n\t"
    "v_mul_f32 v144, v144, v246\n\t"
    "v_mul_f32 v146, v146, v247\n\t"
    "v_mul_f32 v148, v148, v246\n\t"
    "v_mul_f32 v150, v150, v247\n\t"
    "s_waitcnt lgkmcnt(8)\n\t"
    "v_pk_mul_f32 v[64:65], v[64:65], v[144:145] op_sel_hi:[1,0]\n\t"
    "v_pk_mul_f32 v[66:67], v[66:67], v[144:145] op_sel_hi:[1,0]\n\t"
    "v_pk_mul_f32 v[68:69], v[68:69], v[144:145] op_sel_hi:[1,0]\n\t"
    "v_pk_mul_f32 v[70:71], v[70:71], v[144:145] op_sel_hi:[1,0]\n\t"
    "v_pk_fma_f32 v[64:65], v[0:1], v[148:149], v[64:65] op_sel_hi:[1,0,1]\n\t"
    "v_pk_fma_f32 v[66:67], v[2:3], v[148:149], v[66:67] op_sel_hi:[1,0,1]\n\t"
    "v_pk_fma_f32 v[68:69], v[4:5], v[148:149], v[68:69] op_sel_hi:[1,0,1]\n\t"
    "v_pk_fma_f32 v[70:71], v[6:7], v[148:149], v[70:71] op_sel_hi:[1,0,1]\n\t"
    "v_cvt_pk_bf16_f32 v64, v64, v65\n\t"
    "v_cvt_pk_bf16_f32 v65, v66, v67\n\t"
    "v_cvt_pk_bf16_f32 v66, v68, v69\n\t"
    "v_cvt_pk_bf16_f32 v67, v70, v71\n\t"
    "ds_write_b64 v176, v[64:65] offset:0\n\t"
    "ds_write_b64 v177, v[66:67] offset:0\n\t"
    "v_pk_mul_f32 v[72:73], v[72:73], v[144:145] op_sel_hi:[1,0]\n\t"
    "v_pk_mul_f32 v[74:75], v[74:75], v[144:145] op_sel_hi:[1,0]\n\t"
    "v_pk_mul_f32 v[76:77], v[76:77], v[144:145] op_sel_hi:[1,0]\n\t"
    "v_pk_mul_f32 v[78:79], v[78:79], v[144:145] op_sel_hi:[1,0]\n\t"
    "v_pk_fma_f32 v[72:73], v[8:9], v[148:149], v[72:73] op_sel_hi:[1,0,1]\n\t"
    "v_pk_fma_f32 v[74:75], v[10:11], v[148:149], v[74:75] op_sel_hi:[1,0,1]\n\t"
    "v_pk_fma_f32 v[76:77], v[12:13], v[148:149], v[76:77] op_sel_hi:[1,0,1]\n\t"
    "v_pk_fma_f32 v[78:79], v[14:15], v[148:149], v[78:79] op_sel_hi:[1,0,1]\n\t"
    "v_cvt_pk_bf16_f32 v72, v72, v73\n\t"
    "v_cvt_pk_bf16_f32 v73, v74, v75\n\t"
    "v_cvt_pk_bf16_f32 v74, v76, v77\n\t"
    "v_cvt_pk_bf16_f32 v75, v78, v79\n\t"
    "ds_write_b64 v178, v[72:73] offset:0\n\t"
    "ds_write_b64 v179, v[74:75] offset:0\n\t"
    "v_pk_mul_f32 v[80:81], v[80:81], v[144:145] op_sel_hi:[1,0]\n\t"
    "v_pk_mul_f32 v[82:83], v[82:83], v[144:145] op_sel_hi:[1,0]\n\t"
    "v_pk_mul_f32 v[84:85], v[84:85], v[144:145] op_sel_hi:[1,0]\n\t"
    "v_pk_mul_f32 v[86:87], v[86:87], v[144:145] op_sel_hi:[1,0]\n\t"
    "v_pk_fma_f32 v[80:81], v[16:17], v[148:149], v[80:81] op_sel_hi:[1,0,1]\n\t"
    "v_pk_fma_f32 v[82:83], v[18:19], v[148:149], v[82:83] op_sel_hi:[1,0,1]\n\t"
    "v_pk_fma_f32 v[84:85], v[20:21], v[148:149], v[84:85] op_sel_hi:[1,0,1]\n\t"
    "v_pk_fma_f32 v[86:87], v[22:23], v[148:149], v[86:87] op_sel_hi:[1,0,1]\n\t"
    "v_cvt_pk_bf16_f32 v80, v80, v81\n\t"
    "v_cvt_pk_bf16_f32 v81, v82, v83\n\t"
    "v_cvt_pk_bf16_f32 v82, v84, v85\n\t"
    "v_cvt_pk_bf16_f32 v83, v86, v87\n\t"
    "ds_write_b64 v180, v[80:81] offset:0\n\t"
    "ds_write_b64 v181, v[82:83] offset:0\n\t"
    "v_pk_mul_f32 v[88:89], v[88:89], v[144:145] op_sel_hi:[1,0]\n\t"
    "v_pk_mul_f32 v[90:91], v[90:91], v[144:145] op_sel_hi:[1,0]\n\t"
    "v_pk_mul_f32 v[92:93], v[92:93], v[144:145] op_sel_hi:[1,0]\n\t"
    "v_pk_mul_f32 v[94:95], v[94:95], v[144:145] op_sel_hi:[1,0]\n\t"
    "v_pk_fma_f32 v[88:89], v[24:25], v[148:149], v[88:89] op_sel_hi:[1,0,1]\n\t"
    "v_pk_fma_f32 v[90:91], v[26:27], v[148:149], v[90:91] op_sel_hi:[1,0,1]\n\t"
    "v_pk_fma_f32 v[92:93], v[28:29], v[148:149], v[92:93] op_sel_hi:[1,0,1]\n\t"
    "v_pk_fma_f32 v[94:95], v[30:31], v[148:149], v[94:95] op_sel_hi:[1,0,1]\n\t"
    "v_cvt_pk_bf16_f32 v88, v88, v89\n\t"
    "v_cvt_pk_bf16_f32 v89, v90, v91\n\t"
    "v_cvt_pk_bf16_f32 v90, v92, v93\n\t"
    "v_cvt_pk_bf16_f32 v91, v94, v95\n\t"
    "ds_write_b64 v182, v[88:89] offset:0\n\t"
    "ds_write_b64 v183, v[90:91] offset:0\n\t"
    "s_waitcnt lgkmcnt(8)\n\t"
    "v_pk_mul_f32 v[96:97], v[96:97], v[146:147] op_sel_hi:[1,0]\n\t"
    "v_pk_mul_f32 v[98:99], v[98:99], v[146:147] op_sel_hi:[1,0]\n\t"
    "v_pk_mul_f32 v[100:101], v[100:101], v[146:147] op_sel_hi:[1,0]\n\t"
    "v_pk_mul_f32 v[102:103], v[102:103], v[146:147] op_sel_hi:[1,0]\n\t"
    "v_pk_fma_f32 v[96:97], v[32:33], v[150:151], v[96:97] op_sel_hi:[1,0,1]\n\t"
    "v_pk_fma_f32 v[98:99], v[34:35], v[150:151], v[98:99] op_sel_hi:[1,0,1]\n\t"
    "v_pk_fma_f32 v[100:101], v[36:37], v[150:151], v[100:101] op_sel_hi:[1,0,1]\n\t"
    "v_pk_fma_f32 v[102:103], v[38:39], v[150:151], v[102:103] op_sel_hi:[1,0,1]\n\t"
    "v_cvt_pk_bf16_f32 v96, v96, v97\n\t"
    "v_cvt_pk_bf16_f32 v97, v98, v99\n\t"
    "v_cvt_pk_bf16_f32 v98, v100, v101\n\t"
    "v_cvt_pk_bf16_f32 v99, v102, v103\n\t"
    "ds_write_b64 v176, v[96:97] offset:4096\n\t"
    "ds_write_b64 v177, v[98:99] offset:4096\n\t"
    "v_pk_mul_f32 v[104:105], v[104:105], v[146:147] op_sel_hi:[1,0]\n\t"
    "v_pk_mul_f32 v[106:107], v[106:107], v[146:147] op_sel_hi:[1,0]\n\t"
    "v_pk_mul_f32 v[108:109], v[108:109], v[146:147] op_sel_hi:[1,0]\n\t"
    "v_pk_mul_f32 v[110:111], v[110:111], v[146:147] op_sel_hi:[1,0]\n\t"
    "v_pk_fma_f32 v[104:105], v[40:41], v[150:151], v[104:105] op_sel_hi:[1,0,1]\n\t"
    "v_pk_fma_f32 v[106:107], v[42:43], v[150:151], v[106:107] op_sel_hi:[1,0,1]\n\t"
    "v_pk_fma_f32 v[108:109], v[44:45], v[150:151], v[108:109] op_sel_hi:[1,0,1]\n\t"
    "v_pk_fma_f32 v[110:111], v[46:47], v[150:151], v[110:111] op_sel_hi:[1,0,1]\n\t"
    "v_cvt_pk_bf16_f32 v104, v104, v105\n\t"
    "v_cvt_pk_bf16_f32 v105, v106, v107\n\t"
    "v_cvt_pk_bf16_f32 v106, v108, v109\n\t"
    "v_cvt_pk_bf16_f32 v107, v110, v111\n\t"
    "ds_write_b64 v178, v[104:105] offset:4096\n\t"
    "ds_write_b64 v179, v[106:107] offset:4096\n\t"
    "s_waitcnt lgkmcnt(4)\n\t"
    "ds_read_b128 v[64:67], v184 offset:0\n\t"
    "ds_read_b128 v[68:71], v185 offset:1024\n\t"
    "ds_read_b128 v[72:75], v186 offset:2048\n\t"
    "ds_read_b128 v[76:79], v187 offset:3072\n\t"
    "v_pk_mul_f32 v[112:113], v[112:113], v[146:147] op_sel_hi:[1,0]\n\t"
    "v_pk_mul_f32 v[114:115], v[114:115], v[146:147] op_sel_hi:[1,0]\n\t"
    "v_pk_mul_f32 v[116:117], v[116:117], v[146:147] op_sel_hi:[1,0]\n\t"
    "v_pk_mul_f32 v[118:119], v[118:119], v[146:147] op_sel_hi:[1,0]\n\t"
    "v_pk_fma_f32 v[112:113], v[48:49], v[150:151], v[112:113] op_sel_hi:[1,0,1]\n\t"
    "v_pk_fma_f32 v[114:115], v[50:51], v[150:151], v[114:115] op_sel_hi:[1,0,1]\n\t"
    "v_pk_fma_f32 v[116:117], v[52:53], v[150:151], v[116:117] op_sel_hi:[1,0,1]\n\t"
    "v_pk_fma_f32 v[118:119], v[54:55], v[150:151], v[118:119] op_sel_hi:[1,0,1]\n\t"
    "v_cvt_pk_bf16_f32 v112, v112, v113\n\t"
    "v_cvt_pk_bf16_f32 v113, v114, v115\n\t"
    "v_cvt_pk_bf16_f32 v114, v116, v117\n\t"
    "v_cvt_pk_bf16_f32 v115, v118, v119\n\t"
    "ds_write_b64 v180, v[112:113] offset:4096\n\t"
    "ds_write_b64 v181, v[114:115] offset:4096\n\t"
    "v_pk_mul_f32 v[120:121], v[120:121], v[146:147] op_sel_hi:[1,0]\n\t"
    "v_pk_mul_f32 v[122:123], v[122:123], v[146:147] op_sel_hi:[1,0]\n\t"
    "v_pk_mul_f32 v[124:125], v[124:125], v[146:147] op_sel_hi:[1,0]\n\t"
    "v_pk_mul_f32 v[126:127], v[126:127], v[146:147] op_sel_hi:[1,0]\n\t"
    "v_pk_fma_f32 v[120:121], v[56:57], v[150:151], v[120:121] op_sel_hi:[1,0,1]\n\t"
    "v_pk_fma_f32 v[122:123], v[58:59], v[150:151], v[122:123] op_sel_hi:[1,0,1]\n\t"
    "v_pk_fma_f32 v[124:125], v[60:61], v[150:151], v[124:125] op_sel_hi:[1,0,1]\n\t"
    "v_pk_fma_f32 v[126:127], v[62:63], v[150:151], v[126:127] op_sel_hi:[1,0,1]\n\t"
    "v_cvt_pk_bf16_f32 v120, v120, v121\n\t"
    "v_cvt_pk_bf16_f32 v121, v122, v123\n\t"
    "v_cvt_pk_bf16_f32 v122, v124, v125\n\t"
    "v_cvt_pk_bf16_f32 v123, v126, v127\n\t"
    "ds_write_b64 v182, v[120:121] offset:4096\n\t"
    "ds_write_b64 v183, v[122:123] offset:4096\n\t"
    "s_waitcnt lgkmcnt(4)\n\t"
    "buffer_store_dwordx4 v[64:67], v240, s[52:55], s76 offen offset:0 nt\n\t"
    "buffer_store_dwordx4 v[68:71], v240, s[52:55], s76 offen offset:1024 nt\n\t"
    "buffer_store_dwordx4 v[72:75], v240, s[52:55], s76 offen offset:2048 nt\n\t"
    "buffer_store_dwordx4 v[76:79], v240, s[52:55], s76 offen offset:3072 nt\n\t"
    "s_waitcnt lgkmcnt(0)\n\t"
    "ds_read_b128 v[96:99], v184 offset:4096\n\t"
    "ds_read_b128 v[100:103], v185 offset:5120\n\t"
    "ds_read_b128 v[104:107], v186 offset:6144\n\t"
    "ds_read_b128 v[108:111], v187 offset:7168\n\t"
    "s_waitcnt lgkmcnt(3)\n\t"
    "buffer_store_dwordx4 v[96:99], v240, s[52:55], s77 offen offset:0 nt\n\t"
    "s_waitcnt lgkmcnt(2)\n\t"
    "buffer_store_dwordx4 v[100:103], v240, s[52:55], s77 offen offset:1024 nt\n\t"
    "s_waitcnt lgkmcnt(1)\n\t"
    "buffer_store_dwordx4 v[104:107], v240, s[52:55], s77 offen offset:2048 nt\n\t"
    "s_waitcnt lgkmcnt(0)\n\t"
    "buffer_store_dwordx4 v[108:111], v240, s[52:55], s77 offen offset:3072 nt\n\t"
    "s_branch L_end_%=\n\t"
    "L_fb0_%=:\n\t"
    "s_nop 7\n\t"
    "s_nop 7\n\t"
    "v_max3_f32 v242, v128, v129, v130\n\t"
    "v_max3_f32 v243, v131, v132, v133\n\t"
    "v_max3_f32 v244, v136, v137, v138\n\t"
    "v_max3_f32 v245, v139, v140, v141\n\t"
    "v_max3_f32 v242, v242, v134, v135\n\t"
    "v_max3_f32 v244, v244, v142, v143\n\t"
    "v_max_f32 v242, v242, v243\n\t"
    "v_max_f32 v244, v244, v245\n\t"
    "v_mov_b32 v243, v242\n\t"
    "v_mov_b32 v245, v244\n\t"
    "s_nop 0\n\t"
    "v_permlane32_swap_b32 v242, v243\n\t"
    "v_permlane32_swap_b32 v244, v245\n\t"
    "v_max_f32 v242, v242, v243\n\t"
    "v_max_f32 v244, v244, v245\n\t"
    "v_mov_b32 v243, v242\n\t"
    "v_mov_b32 v245, v244\n\t"
    "s_nop 0\n\t"
    "v_permlane16_swap_b32 v242, v243\n\t"
    "v_permlane16_swap_b32 v244, v245\n\t"
    "v_max_f32 v242, v242, v243\n\t"
    "v_max_f32 v244, v244, v245\n\t"
    "v_min_f32 v243, 0x5d800000, v212\n\t"
    "v_log_f32 v243, v243\n\t"
    "s_nop 0\n\t"
    "v_add_f32 v243, 0xc1400000, v243\n\t"
    "v_max3_f32 v242, v242, v243, 0\n\t"
    "v_ceil_f32 v242, v242\n\t"
    "v_exp_f32_e64 v243, -v242\n\t"
    "v_sub_f32 v228, v228, v242\n\t"
    "v_sub_f32 v160, v160, v242\n\t"
    "v_sub_f32 v161, v161, v242\n\t"
    "v_sub_f32 v162, v162, v242\n\t"
    "v_sub_f32 v163, v163, v242\n\t"
    "v_sub_f32 v164, v164, v242\n\t"
    "v_sub_f32 v165, v165, v242\n\t"
    "v_sub_f32 v166, v166, v242\n\t"
    "v_sub_f32 v167, v167, v242\n\t"
    "v_sub_f32 v246, v128, v242\n\t"
    "v_sub_f32 v247, v129, v242\n\t"
    "v_exp_f32 v246, v246\n\t"
    "v_exp_f32 v247, v247\n\t"
    "s_nop 0\n\t"
    "v_cvt_pk_bf16_f32 v192, v246, v247\n\t"
    "v_sub_f32 v246, v130, v242\n\t"
    "v_sub_f32 v247, v131, v242\n\t"
    "v_exp_f32 v246, v246\n\t"
    "v_exp_f32 v247, v247\n\t"
    "s_nop 0\n\t"
    "v_cvt_pk_bf16_f32 v193, v246, v247\n\t"
    "v_sub_f32 v246, v132, v242\n\t"
    "v_sub_f32 v247, v133, v242\n\t"
    "v_exp_f32 v246, v246\n\t"
    "v_exp_f32 v247, v247\n\t"
    "s_nop 0\n\t"
    "v_cvt_pk_bf16_f32 v194, v246, v247\n\t"
    "v_sub_f32 v246, v134, v242\n\t"
    "v_sub_f32 v247, v135, v242\n\t"
    "v_exp_f32 v246, v246\n\t"
    "v_exp_f32 v247, v247\n\t"
    "s_nop 0\n\t"
    "v_cvt_pk_bf16_f32 v195, v246, v247\n\t"
    "s_nop 1\n\t"
    "v_mfma_f32_16x16x32_bf16 v[250:253], a[128:131], v[192:195], 0\n\t"
    "v_mul_f32 v0, v0, v243\n\t"
    "v_mul_f32 v1, v1, v243\n\t"
    "v_mul_f32 v2, v2, v243\n\t"
    "v_mul_f32 v3, v3, v243\n\t"
    "v_mul_f32 v4, v4, v243\n\t"
    "v_mul_f32 v5, v5, v243\n\t"
    "v_mul_f32 v6, v6, v243\n\t"
    "v_mul_f32 v7, v7, v243\n\t"
    "v_mul_f32 v8, v8, v243\n\t"
    "v_mul_f32 v9, v9, v243\n\t"
    "v_mul_f32 v10, v10, v243\n\t"
    "v_mul_f32 v11, v11, v243\n\t"
    "v_mul_f32 v12, v12, v243\n\t"
    "v_mul_f32 v13, v13, v243\n\t"
    "v_mul_f32 v14, v14, v243\n\t"
    "v_mul_f32 v15, v15, v243\n\t"
    "v_mul_f32 v16, v16, v243\n\t"
    "v_mul_f32 v17, v17, v243\n\t"
    "v_mul_f32 v18, v18, v243\n\t"
    "v_mul_f32 v19, v19, v243\n\t"
    "v_mul_f32 v20, v20, v243\n\t"
    "v_mul_f32 v21, v21, v243\n\t"
    "v_mul_f32 v22, v22, v243\n\t"
    "v_mul_f32 v23, v23, v243\n\t"
    "v_mul_f32 v24, v24, v243\n\t"
    "v_mul_f32 v25, v25, v243\n\t"
    "v_mul_f32 v26, v26, v243\n\t"
    "v_mul_f32 v27, v27, v243\n\t"
    "v_mul_f32 v28, v28, v243\n\t"
    "v_mul_f32 v29, v29, v243\n\t"
    "v_mul_f32 v30, v30, v243\n\t"
    "v_mul_f32 v31, v31, v243\n\t"
    "v_cmp_gt_f32 vcc, 0x71800000, v212\n\t"
    "v_mul_f32 v212, v212, v243\n\t"
    "s_nop 0\n\t"
    "v_cndmask_b32 v212, v250, v212, vcc\n\t"
    "v_min_f32 v245, 0x5d800000, v216\n\t"
    "v_log_f32 v245, v245\n\t"
    "s_nop 0\n\t"
    "v_add_f32 v245, 0xc1400000, v245\n\t"
    "v_max3_f32 v244, v244, v245, 0\n\t"
    "v_ceil_f32 v244, v244\n\t"
    "v_exp_f32_e64 v245, -v244\n\t"
    "v_sub_f32 v229, v229, v244\n\t"
    "v_sub_f32 v168, v168, v244\n\t"
    "v_sub_f32 v169, v169, v244\n\t"
    "v_sub_f32 v170, v170, v244\n\t"
    "v_sub_f32 v171, v171, v244\n\t"
    "v_sub_f32 v172, v172, v244\n\t"
    "v_sub_f32 v173, v173, v244\n\t"
    "v_sub_f32 v174, v174, v244\n\t"
    "v_sub_f32 v175, v175, v244\n\t"
    "v_sub_f32 v246, v136, v244\n\t"
    "v_sub_f32 v247, v137, v244\n\t"
    "v_exp_f32 v246, v246\n\t"
    "v_exp_f32 v247, v247\n\t"
    "s_nop 0\n\t"
    "v_cvt_pk_bf16_f32 v196, v246, v247\n\t"
    "v_sub_f32 v246, v138, v244\n\t"
    "v_sub_f32 v247, v139, v244\n\t"
    "v_exp_f32 v246, v246\n\t"
    "v_exp_f32 v247, v247\n\t"
    "s_nop 0\n\t"
    "v_cvt_pk_bf16_f32 v197, v246, v247\n\t"
    "v_sub_f32 v246, v140, v244\n\t"
    "v_sub_f32 v247, v141, v244\n\t"
    "v_exp_f32 v246, v246\n\t"
    "v_exp_f32 v247, v247\n\t"
    "s_nop 0\n\t"
    "v_cvt_pk_bf16_f32 v198, v246, v247\n\t"
    "v_sub_f32 v246, v142, v244\n\t"
    "v_sub_f32 v247, v143, v244\n\t"
    "v_exp_f32 v246, v246\n\t"
    "v_exp_f32 v247, v247\n\t"
    "s_nop 0\n\t"
    "v_cvt_pk_bf16_f32 v199, v246, v247\n\t"
    "s_nop 1\n\t"
    "v_mfma_f32_16x16x32_bf16 v[250:253], a[128:131], v[196:199], 0\n\t"
    "v_mul_f32 v32, v32, v245\n\t"
    "v_mul_f32 v33, v33, v245\n\t"
    "v_mul_f32 v34, v34, v245\n\t"
    "v_mul_f32 v35, v35, v245\n\t"
    "v_mul_f32 v36, v36, v245\n\t"
    "v_mul_f32 v37, v37, v245\n\t"
    "v_mul_f32 v38, v38, v245\n\t"
    "v_mul_f32 v39, v39, v245\n\t"
    "v_mul_f32 v40, v40, v245\n\t"
    "v_mul_f32 v41, v41, v245\n\t"
    "v_mul_f32 v42, v42, v245\n\t"
    "v_mul_f32 v43, v43, v245\n\t"
    "v_mul_f32 v44, v44, v245\n\t"
    "v_mul_f32 v45, v45, v245\n\t"
    "v_mul_f32 v46, v46, v245\n\t"
    "v_mul_f32 v47, v47, v245\n\t"
    "v_mul_f32 v48, v48, v245\n\t"
    "v_mul_f32 v49, v49, v245\n\t"
    "v_mul_f32 v50, v50, v245\n\t"
    "v_mul_f32 v51, v51, v245\n\t"
    "v_mul_f32 v52, v52, v245\n\t"
    "v_mul_f32 v53, v53, v245\n\t"
    "v_mul_f32 v54, v54, v245\n\t"
    "v_mul_f32 v55, v55, v245\n\t"
    "v_mul_f32 v56, v56, v245\n\t"
    "v_mul_f32 v57, v57, v245\n\t"
    "v_mul_f32 v58, v58, v245\n\t"
    "v_mul_f32 v59, v59, v245\n\t"
    "v_mul_f32 v60, v60, v245\n\t"
    "v_mul_f32 v61, v61, v245\n\t"
    "v_mul_f32 v62, v62, v245\n\t"
    "v_mul_f32 v63, v63, v245\n\t"
    "v_cmp_gt_f32 vcc, 0x71800000, v216\n\t"
    "v_mul_f32 v216, v216, v245\n\t"
    "s_nop 0\n\t"
    "v_cndmask_b32 v216, v250, v216, vcc\n\t"
    "s_nop 1\n\t"
    "s_setpc_b64 s[66:67]\n\t"
    "L_fb1_%=:\n\t"
    "s_nop 7\n\t"
    "s_nop 7\n\t"
    "v_max3_f32 v242, v144, v145, v146\n\t"
    "v_max3_f32 v243, v147, v148, v149\n\t"
    "v_max3_f32 v244, v152, v153, v154\n\t"
    "v_max3_f32 v245, v155, v156, v157\n\t"
    "v_max3_f32 v242, v242, v150, v151\n\t"
    "v_max3_f32 v244, v244, v158, v159\n\t"
    "v_max_f32 v242, v242, v243\n\t"
    "v_max_f32 v244, v244, v245\n\t"
    "v_mov_b32 v243, v242\n\t"
    "v_mov_b32 v245, v244\n\t"
    "s_nop 0\n\t"
    "v_permlane32_swap_b32 v242, v243\n\t"
    "v_permlane32_swap_b32 v244, v245\n\t"
    "v_max_f32 v242, v242, v243\n\t"
    "v_max_f32 v244, v244, v245\n\t"
    "v_mov_b32 v243, v242\n\t"
    "v_mov_b32 v245, v244\n\t"
    "s_nop 0\n\t"
    "v_permlane16_swap_b32 v242, v243\n\t"
    "v_permlane16_swap_b32 v244, v245\n\t"
    "v_max_f32 v242, v242, v243\n\t"
    "v_max_f32 v244, v244, v245\n\t"
    "v_min_f32 v243, 0x5d800000, v220\n\t"
    "v_log_f32 v243, v243\n\t"
    "s_nop 0\n\t"
    "v_add_f32 v243, 0xc1400000, v243\n\t"
    "v_max3_f32 v242, v242, v243, 0\n\t"
    "v_ceil_f32 v242, v242\n\t"
    "v_exp_f32_e64 v243, -v242\n\t"
    "v_sub_f32 v230, v230, v242\n\t"
    "v_sub_f32 v176, v176, v242\n\t"
    "v_sub_f32 v177, v177, v242\n\t"
    "v_sub_f32 v178, v178, v242\n\t"
    "v_sub_f32 v179, v179, v242\n\t"
    "v_sub_f32 v180, v180, v242\n\t"
    "v_sub_f32 v181, v181, v242\n\t"
    "v_sub_f32 v182, v182, v242\n\t"
    "v_sub_f32 v183, v183, v242\n\t"
    "v_sub_f32 v246, v144, v242\n\t"
    "v_sub_f32 v247, v145, v242\n\t"
    "v_exp_f32 v246, v246\n\t"
    "v_exp_f32 v247, v247\n\t"
    "s_nop 0\n\t"
    "v_cvt_pk_bf16_f32 v200, v246, v247\n\t"
    "v_sub_f32 v246, v146, v242\n\t"
    "v_sub_f32 v247, v147, v242\n\t"
    "v_exp_f32 v246, v246\n\t"
    "v_exp_f32 v247, v247\n\t"
    "s_nop 0\n\t"
    "v_cvt_pk_bf16_f32 v201, v246, v247\n\t"
    "v_sub_f32 v246, v148, v242\n\t"
    "v_sub_f32 v247, v149, v242\n\t"
    "v_exp_f32 v246, v246\n\t"
    "v_exp_f32 v247, v247\n\t"
    "s_nop 0\n\t"
    "v_cvt_pk_bf16_f32 v202, v246, v247\n\t"
    "v_sub_f32 v246, v150, v242\n\t"
    "v_sub_f32 v247, v151, v242\n\t"
    "v_exp_f32 v246, v246\n\t"
    "v_exp_f32 v247, v247\n\t"
    "s_nop 0\n\t"
    "v_cvt_pk_bf16_f32 v203, v246, v247\n\t"
    "s_nop 1\n\t"
    "v_mfma_f32_16x16x32_bf16 v[250:253], a[128:131], v[200:203], 0\n\t"
    "v_mul_f32 v64, v64, v243\n\t"
    "v_mul_f32 v65, v65, v243\n\t"
    "v_mul_f32 v66, v66, v243\n\t"
    "v_mul_f32 v67, v67, v243\n\t"
    "v_mul_f32 v68, v68, v243\n\t"
    "v_mul_f32 v69, v69, v243\n\t"
    "v_mul_f32 v70, v70, v243\n\t"
    "v_mul_f32 v71, v71, v243\n\t"
    "v_mul_f32 v72, v72, v243\n\t"
    "v_mul_f32 v73, v73, v243\n\t"
    "v_mul_f32 v74, v74, v243\n\t"
    "v_mul_f32 v75, v75, v243\n\t"
    "v_mul_f32 v76, v76, v243\n\t"
    "v_mul_f32 v77, v77, v243\n\t"
    "v_mul_f32 v78, v78, v243\n\t"
    "v_mul_f32 v79, v79, v243\n\t"
    "v_mul_f32 v80, v80, v243\n\t"
    "v_mul_f32 v81, v81, v243\n\t"
    "v_mul_f32 v82, v82, v243\n\t"
    "v_mul_f32 v83, v83, v243\n\t"
    "v_mul_f32 v84, v84, v243\n\t"
    "v_mul_f32 v85, v85, v243\n\t"
    "v_mul_f32 v86, v86, v243\n\t"
    "v_mul_f32 v87, v87, v243\n\t"
    "v_mul_f32 v88, v88, v243\n\t"
    "v_mul_f32 v89, v89, v243\n\t"
    "v_mul_f32 v90, v90, v243\n\t"
    "v_mul_f32 v91, v91, v243\n\t"
    "v_mul_f32 v92, v92, v243\n\t"
    "v_mul_f32 v93, v93, v243\n\t"
    "v_mul_f32 v94, v94, v243\n\t"
    "v_mul_f32 v95, v95, v243\n\t"
    "v_cmp_gt_f32 vcc, 0x71800000, v220\n\t"
    "v_mul_f32 v220, v220, v243\n\t"
    "s_nop 0\n\t"
    "v_cndmask_b32 v220, v250, v220, vcc\n\t"
    "v_min_f32 v245, 0x5d800000, v224\n\t"
    "v_log_f32 v245, v245\n\t"
    "s_nop 0\n\t"
    "v_add_f32 v245, 0xc1400000, v245\n\t"
    "v_max3_f32 v244, v244, v245, 0\n\t"
    "v_ceil_f32 v244, v244\n\t"
    "v_exp_f32_e64 v245, -v244\n\t"
    "v_sub_f32 v231, v231, v244\n\t"
    "v_sub_f32 v184, v184, v244\n\t"
    "v_sub_f32 v185, v185, v244\n\t"
    "v_sub_f32 v186, v186, v244\n\t"
    "v_sub_f32 v187, v187, v244\n\t"
    "v_sub_f32 v188, v188, v244\n\t"
    "v_sub_f32 v189, v189, v244\n\t"
    "v_sub_f32 v190, v190, v244\n\t"
    "v_sub_f32 v191, v191, v244\n\t"
    "v_sub_f32 v246, v152, v244\n\t"
    "v_sub_f32 v247, v153, v244\n\t"
    "v_exp_f32 v246, v246\n\t"
    "v_exp_f32 v247, v247\n\t"
    "s_nop 0\n\t"
    "v_cvt_pk_bf16_f32 v204, v246, v247\n\t"
    "v_sub_f32 v246, v154, v244\n\t"
    "v_sub_f32 v247, v155, v244\n\t"
    "v_exp_f32 v246, v246\n\t"
    "v_exp_f32 v247, v247\n\t"
    "s_nop 0\n\t"
    "v_cvt_pk_bf16_f32 v205, v246, v247\n\t"
    "v_sub_f32 v246, v156, v244\n\t"
    "v_sub_f32 v247, v157, v244\n\t"
    "v_exp_f32 v246, v246\n\t"
    "v_exp_f32 v247, v247\n\t"
    "s_nop 0\n\t"
    "v_cvt_pk_bf16_f32 v206, v246, v247\n\t"
    "v_sub_f32 v246, v158, v244\n\t"
    "v_sub_f32 v247, v159, v244\n\t"
    "v_exp_f32 v246, v246\n\t"
    "v_exp_f32 v247, v247\n\t"
    "s_nop 0\n\t"
    "v_cvt_pk_bf16_f32 v207, v246, v247\n\t"
    "s_nop 1\n\t"
    "v_mfma_f32_16x16x32_bf16 v[250:253], a[128:131], v[204:207], 0\n\t"
    "v_mul_f32 v96, v96, v245\n\t"
    "v_mul_f32 v97, v97, v245\n\t"
    "v_mul_f32 v98, v98, v245\n\t"
    "v_mul_f32 v99, v99, v245\n\t"
    "v_mul_f32 v100, v100, v245\n\t"
    "v_mul_f32 v101, v101, v245\n\t"
    "v_mul_f32 v102, v102, v245\n\t"
    "v_mul_f32 v103, v103, v245\n\t"
    "v_mul_f32 v104, v104, v245\n\t"
    "v_mul_f32 v105, v105, v245\n\t"
    "v_mul_f32 v106, v106, v245\n\t"
    "v_mul_f32 v107, v107, v245\n\t"
    "v_mul_f32 v108, v108, v245\n\t"
    "v_mul_f32 v109, v109, v245\n\t"
    "v_mul_f32 v110, v110, v245\n\t"
    "v_mul_f32 v111, v111, v245\n\t"
    "v_mul_f32 v112, v112, v245\n\t"
    "v_mul_f32 v113, v113, v245\n\t"
    "v_mul_f32 v114, v114, v245\n\t"
    "v_mul_f32 v115, v115, v245\n\t"
    "v_mul_f32 v116, v116, v245\n\t"
    "v_mul_f32 v117, v117, v245\n\t"
    "v_mul_f32 v118, v118, v245\n\t"
    "v_mul_f32 v119, v119, v245\n\t"
    "v_mul_f32 v120, v120, v245\n\t"
    "v_mul_f32 v121, v121, v245\n\t"
    "v_mul_f32 v122, v122, v245\n\t"
    "v_mul_f32 v123, v123, v245\n\t"
    "v_mul_f32 v124, v124, v245\n\t"
    "v_mul_f32 v125, v125, v245\n\t"
    "v_mul_f32 v126, v126, v245\n\t"
    "v_mul_f32 v127, v127, v245\n\t"
    "v_cmp_gt_f32 vcc, 0x71800000, v224\n\t"
    "v_mul_f32 v224, v224, v245\n\t"
    "s_nop 0\n\t"
    "v_cndmask_b32 v224, v250, v224, vcc\n\t"
    "s_nop 1\n\t"
    "s_setpc_b64 s[66:67]\n\t"
    "L_stub0_%=:\n\t"
    "s_call_b64 s[66:67], L_fb0_%=\n\t"
    "s_branch L_ret0_%=\n\t"
    "L_stub1_%=:\n\t"
    "s_call_b64 s[66:67], L_fb1_%=\n\t"
    "s_branch L_ret1_%=\n\t"
    "L_stub2_%=:\n\t"
    "s_call_b64 s[66:67], L_fb0_%=\n\t"
    "s_branch L_ret2_%=\n\t"
    "L_stub3_%=:\n\t"
    "s_call_b64 s[66:67], L_fb0_%=\n\t"
    "s_branch L_ret3_%=\n\t"
    "L_end_%=:\n\t"
    :: "s"(__builtin_amdgcn_kernarg_segment_ptr()), "s"(wid), "s"(blockIdx.x) : "v0", "v1", "v2", "v3", "v4", "v5", "v6", "v7", "v8", "v9", "v10", "v11", "v12", "v13", "v14", "v15", "v16", "v17", "v18", "v19", "v20", "v21", "v22", "v23", "v24", "v25", "v26", "v27", "v28", "v29", "v30", "v31", "v32", "v33", "v34", "v35", "v36", "v37", "v38", "v39", "v40", "v41", "v42", "v43", "v44", "v45", "v46", "v47", "v48", "v49", "v50", "v51", "v52", "v53", "v54", "v55", "v56", "v57", "v58", "v59", "v60", "v61", "v62", "v63", "v64", "v65", "v66", "v67", "v68", "v69", "v70", "v71", "v72", "v73", "v74", "v75", "v76", "v77", "v78", "v79", "v80", "v81", "v82", "v83", "v84", "v85", "v86", "v87", "v88", "v89", "v90", "v91", "v92", "v93", "v94", "v95", "v96", "v97", "v98", "v99", "v100", "v101", "v102", "v103", "v104", "v105", "v106", "v107", "v108", "v109", "v110", "v111", "v112", "v113", "v114", "v115", "v116", "v117", "v118", "v119", "v120", "v121", "v122", "v123", "v124", "v125", "v126", "v127", "v128", "v129", "v130", "v131", "v132", "v133", "v134", "v135", "v136", "v137", "v138", "v139", "v140", "v141", "v142", "v143", "v144", "v145", "v146", "v147", "v148", "v149", "v150", "v151", "v152", "v153", "v154", "v155", "v156", "v157", "v158", "v159", "v160", "v161", "v162", "v163", "v164", "v165", "v166", "v167", "v168", "v169", "v170", "v171", "v172", "v173", "v174", "v175", "v176", "v177", "v178", "v179", "v180", "v181", "v182", "v183", "v184", "v185", "v186", "v187", "v188", "v189", "v190", "v191", "v192", "v193", "v194", "v195", "v196", "v197", "v198", "v199", "v200", "v201", "v202", "v203", "v204", "v205", "v206", "v207", "v208", "v209", "v210", "v211", "v212", "v213", "v214", "v215", "v216", "v217", "v218", "v219", "v220", "v221", "v222", "v223", "v224", "v225", "v226", "v227", "v228", "v229", "v230", "v231", "v232", "v233", "v234", "v235", "v236", "v237", "v238", "v239", "v240", "v241", "v242", "v243", "v244", "v245", "v246", "v247", "v248", "v249", "v250", "v251", "v252", "v253", "v254", "v255", "a0", "a1", "a2", "a3", "a4", "a5", "a6", "a7", "a8", "a9", "a10", "a11", "a12", "a13", "a14", "a15", "a16", "a17", "a18", "a19", "a20", "a21", "a22", "a23", "a24", "a25", "a26", "a27", "a28", "a29", "a30", "a31", "a32", "a33", "a34", "a35", "a36", "a37", "a38", "a39", "a40", "a41", "a42", "a43", "a44", "a45", "a46", "a47", "a48", "a49", "a50", "a51", "a52", "a53", "a54", "a55", "a56", "a57", "a58", "a59", "a60", "a61", "a62", "a63", "a64", "a65", "a66", "a67", "a68", "a69", "a70", "a71", "a72", "a73", "a74", "a75", "a76", "a77", "a78", "a79", "a80", "a81", "a82", "a83", "a84", "a85", "a86", "a87", "a88", "a89", "a90", "a91", "a92", "a93", "a94", "a95", "a96", "a97", "a98", "a99", "a100", "a101", "a102", "a103", "a104", "a105", "a106", "a107", "a108", "a109", "a110", "a111", "a112", "a113", "a114", "a115", "a116", "a117", "a118", "a119", "a120", "a121", "a122", "a123", "a124", "a125", "a126", "a127", "a128", "a129", "a130", "a131", "a132", "a133", "a134", "a135", "a136", "a137", "a138", "a139", "s14", "s15", "s16", "s17", "s18", "s19", "s20", "s21", "s22", "s23", "s24", "s25", "s26", "s27", "s28", "s29", "s30", "s31", "s33", "s34", "s35", "s36", "s37", "s38", "s39", "s40", "s41", "s42", "s43", "s44", "s45", "s46", "s47", "s48", "s49", "s50", "s51", "s52", "s53", "s54", "s55", "s56", "s57", "s58", "s59", "s60", "s61", "s62", "s63", "s64", "s65", "s66", "s67", "s68", "s69", "s70", "s71", "s72", "s73", "s74", "s75", "s76", "s77", "s78", "s79", "s80", "s81", "s82", "s83", "s84", "s85", "s86", "s87", "s88", "s89", "s90", "s91", "s92", "s93", "s94", "s95", "s96", "s97", "s98", "s99", "vcc", "scc", "memory");
}

extern "C" void kernel_launch(void* const* d_in, const int* in_sizes, int n_in, void* d_out, int out_size, void* d_ws, size_t ws_size, hipStream_t stream) {
  (void)in_sizes; (void)n_in; (void)out_size; (void)ws_size; (void)d_ws;
  (void)hipFuncSetAttribute((const void*)attn_causal_fwd, hipFuncAttributeMaxDynamicSharedMemorySize, 163840);
  hipLaunchKernelGGL(attn_causal_fwd, dim3(256), dim3(256), 163840, stream, (const void*)d_in[0], (const void*)d_in[1], (const void*)d_in[2], d_out);
}
```
